```python
import torch

def _softmax(x, axis=-1):
    return torch.softmax(x, dim=axis)
def _gelu(x):
    return torch.nn.functional.gelu(x, approximate="tanh")
def _silu(x):
    return torch.nn.functional.silu(x)
def _sigmoid(x):
    return torch.sigmoid(x)
def _layernorm(x, axis=-1, eps=1e-5):
    m = x.mean(dim=axis, keepdim=True)
    v = x.var(dim=axis, keepdim=True, unbiased=False)
    return (x - m) / torch.sqrt(v + eps)
def _scatter_add(tgt, idx, src, axis=0):
    return tgt.clone().index_add_(axis, idx, src)
def _segment_sum(src, seg, n):
    out = torch.zeros((n,) + src.shape[1:], dtype=src.dtype)
    return out.index_add_(0, seg, src)
def _pad(x, pw):
    flat = tuple(v for pair in reversed(pw) for v in pair)
    return torch.nn.functional.pad(x, flat)

kernel_name = 'concat_heads__gqa_block_cached__gqa_block_cached__near_ridge'

def setup_inputs():

    _w = (lambda a: (lambda t: t.to(torch.bfloat16) if t.is_floating_point() else t)(torch.as_tensor(a)))
    def _A_setup():
        B, Sq, D = 8, 96, 512
        Hq, Hkv, Sk, Dh = 32, 32, 512, 128
        assert Hq % Hkv == 0, (Hq, Hkv)
        return {
            "x": _w((lambda *s: torch.randn(*s, dtype=torch.bfloat16) * 0.8)(B, Sq, D)),
            "k_cache": _w((lambda *s: torch.randn(*s, dtype=torch.bfloat16) * 0.8)(B, Hkv, Sk, Dh)),
            "v_cache": _w((lambda *s: torch.randn(*s, dtype=torch.bfloat16) * 0.05)(B, Hkv, Sk, Dh)),
            "wq": _w((lambda *s: torch.randn(*s, dtype=torch.bfloat16) * 0.8)(D, Hq * Dh) * (D ** -0.5)),
            "wo": _w((lambda *s: torch.randn(*s, dtype=torch.bfloat16) * 0.05)(Hq * Dh, D) * ((Hq * Dh) ** -0.5)),
        }
    _A_in = _A_setup()

    def _B_setup():
        B, Sq, D = 8, 96, 512
        Hq, Hkv, Sk, Dh = 32, 32, 512, 128
        assert Hq % Hkv == 0, (Hq, Hkv)
        return {
            "x": _w((lambda *s: torch.randn(*s, dtype=torch.bfloat16) * 0.8)(B, Sq, D)),
            "k_cache": _w((lambda *s: torch.randn(*s, dtype=torch.bfloat16) * 0.8)(B, Hkv, Sk, Dh)),
            "v_cache": _w((lambda *s: torch.randn(*s, dtype=torch.bfloat16) * 0.05)(B, Hkv, Sk, Dh)),
            "wq": _w((lambda *s: torch.randn(*s, dtype=torch.bfloat16) * 0.8)(D, Hq * Dh) * (D ** -0.5)),
            "wo": _w((lambda *s: torch.randn(*s, dtype=torch.bfloat16) * 0.05)(Hq * Dh, D) * ((Hq * Dh) ** -0.5)),
        }
    _B_in = _B_setup()

    return {
        "A_x": _A_in["x"],
        "A_k_cache": _A_in["k_cache"],
        "A_v_cache": _A_in["v_cache"],
        "A_wq": _A_in["wq"],
        "A_wo": _A_in["wo"],
        "B_x": _B_in["x"],
        "B_k_cache": _B_in["k_cache"],
        "B_v_cache": _B_in["v_cache"],
        "B_wq": _B_in["wq"],
        "B_wo": _B_in["wo"],
    }

def reference(A_x, A_k_cache, A_v_cache, A_wq, A_wo, B_x, B_k_cache, B_v_cache, B_wq, B_wo):
    def _A_ref(x, k_cache, v_cache, wq, wo):
        B, Sq, D = x.shape
        Hq, Dh = 32, 128
        Hkv = k_cache.shape[1]
        rep = Hq // Hkv
        q = (lambda x, *d: x.permute(*d))((x @ wq).reshape(B, Sq, Hq, Dh), 0, 2, 1, 3)
        k = (lambda x, n, axis: torch.repeat_interleave(x, n, dim=axis))(k_cache, rep, axis=1)
        v = (lambda x, n, axis: torch.repeat_interleave(x, n, dim=axis))(v_cache, rep, axis=1)
        scores = torch.einsum("bhqd,bhkd->bhqk", q, k) * (Dh ** -0.5)
        p = _softmax(scores, axis=-1)
        o = torch.einsum("bhqk,bhkd->bhqd", p, v)
        return (lambda x, *d: x.permute(*d))(o, 0, 2, 1, 3).reshape(B, Sq, Hq * Dh) @ wo

    def _B_ref(x, k_cache, v_cache, wq, wo):
        B, Sq, D = x.shape
        Hq, Dh = 32, 128
        Hkv = k_cache.shape[1]
        rep = Hq // Hkv
        q = (lambda x, *d: x.permute(*d))((x @ wq).reshape(B, Sq, Hq, Dh), 0, 2, 1, 3)
        k = (lambda x, n, axis: torch.repeat_interleave(x, n, dim=axis))(k_cache, rep, axis=1)
        v = (lambda x, n, axis: torch.repeat_interleave(x, n, dim=axis))(v_cache, rep, axis=1)
        scores = torch.einsum("bhqd,bhkd->bhqk", q, k) * (Dh ** -0.5)
        p = _softmax(scores, axis=-1)
        o = torch.einsum("bhqk,bhkd->bhqd", p, v)
        return (lambda x, *d: x.permute(*d))(o, 0, 2, 1, 3).reshape(B, Sq, Hq * Dh) @ wo

    a = _A_ref(A_x, A_k_cache, A_v_cache, A_wq, A_wo)
    b = _B_ref(B_x, B_k_cache, B_v_cache, B_wq, B_wo)
    return torch.concatenate([a, b], axis=-1)
```

```cpp
#include <hip/hip_runtime.h>
#include <hip/hip_bf16.h>
#include <stdint.h>

typedef unsigned short u16;
using bf16x8 = __attribute__((ext_vector_type(8))) short;
using s16x4  = __attribute__((ext_vector_type(4))) short;
using f32x16 = __attribute__((ext_vector_type(16))) float;
using f32x4  = __attribute__((ext_vector_type(4))) float;
using u32x4  = __attribute__((ext_vector_type(4))) unsigned;
using u32x2  = __attribute__((ext_vector_type(2))) unsigned;
typedef short v4i16_t __attribute__((ext_vector_type(4)));
typedef __attribute__((address_space(3))) char* lds_ptr;

constexpr int NB = 8, SQ = 96, DM = 512, NH = 32, SK = 512, DH = 128;
constexpr int MROWS = NB * SQ;
constexpr int KO = NH * DH;

__device__ __forceinline__ unsigned swz_b(unsigned row) { return ((row & 3) << 2) | ((row >> 2) & 3); }
__device__ __forceinline__ unsigned off_b(unsigned row, unsigned ch) { return 256u * row + 16u * (ch ^ swz_b(row)); }

__device__ __forceinline__ void glds16(const void* g, unsigned lds_dst) {
  unsigned keep;
  asm volatile("s_mov_b32 %0, m0\n\ts_mov_b32 m0, %2\n\ts_nop 0\n\tglobal_load_lds_dwordx4 %1, off\n\ts_mov_b32 m0, %0"
               : "=&s"(keep) : "v"(g), "s"(lds_dst) : "memory");
}
__device__ __forceinline__ void glds16_nt(const void* g, unsigned lds_dst) {
  unsigned keep;
  asm volatile("s_mov_b32 %0, m0\n\ts_mov_b32 m0, %2\n\ts_nop 0\n\tglobal_load_lds_dwordx4 %1, off nt\n\ts_mov_b32 m0, %0"
               : "=&s"(keep) : "v"(g), "s"(lds_dst) : "memory");
}
__device__ __forceinline__ void glds16x4_nt(const void* sbase, unsigned v0, unsigned v1, unsigned v2, unsigned v3, unsigned lds_dst) {
  unsigned keep;
  asm volatile("s_mov_b32 %0, m0\n\ts_mov_b32 m0, %6\n\ts_nop 0\n\t"
               "global_load_lds_dwordx4 %1, %5 nt\n\t"
               "global_load_lds_dwordx4 %2, %5 offset:1024 nt\n\t"
               "global_load_lds_dwordx4 %3, %5 offset:2048 nt\n\t"
               "global_load_lds_dwordx4 %4, %5 offset:3072 nt\n\t"
               "s_mov_b32 m0, %0"
               : "=&s"(keep) : "v"(v0), "v"(v1), "v"(v2), "v"(v3), "s"(sbase), "s"(lds_dst) : "memory");
}
template <int N> __device__ __forceinline__ void wait_vm() { asm volatile("s_waitcnt vmcnt(%0)" :: "i"(N) : "memory"); }
__device__ __forceinline__ void bar() { asm volatile("s_waitcnt lgkmcnt(0)\n\ts_barrier" ::: "memory"); }

__device__ __forceinline__ s16x4 vtr(unsigned a) {
  return __builtin_bit_cast(s16x4, __builtin_amdgcn_ds_read_tr16_b64_v4i16((__attribute__((address_space(3))) v4i16_t*)(uintptr_t)a));
}
__device__ __forceinline__ bf16x8 ldsr128(unsigned a) {
  return *(const __attribute__((address_space(3))) bf16x8*)(uintptr_t)a;
}
typedef __bf16 bf16x2_t __attribute__((ext_vector_type(2)));
typedef float f32x2_t __attribute__((ext_vector_type(2)));
__device__ __forceinline__ unsigned cvtpk(float lo, float hi) {
  return __builtin_bit_cast(unsigned, __builtin_convertvector((f32x2_t){lo, hi}, bf16x2_t));
}
__device__ __forceinline__ float swap32_max(float v) {
  auto rr = __builtin_amdgcn_permlane32_swap(__float_as_uint(v), __float_as_uint(v), false, false);
  return fmaxf(__uint_as_float(rr[0]), __uint_as_float(rr[1]));
}
__device__ __forceinline__ float swap32_sum(float v) {
  auto rr = __builtin_amdgcn_permlane32_swap(__float_as_uint(v), __float_as_uint(v), false, false);
  return __uint_as_float(rr[0]) + __uint_as_float(rr[1]);
}
#define MFMA(a, b, c) __builtin_amdgcn_mfma_f32_32x32x16_bf16(a, b, c, 0, 0, 0)
#define PK8(L, H) (bf16x8){L[0], L[1], L[2], L[3], H[0], H[1], H[2], H[3]}

constexpr int SLOT = 16384, NSLOT = 5;


__global__ __launch_bounds__(384, 3) void attn_kernel(
    const u16* __restrict__ xA, const u16* __restrict__ kA, const u16* __restrict__ vA, const u16* __restrict__ wqA,
    const u16* __restrict__ xB, const u16* __restrict__ kB, const u16* __restrict__ vB, const u16* __restrict__ wqB,
    u16* __restrict__ ows) {
  __shared__ __attribute__((aligned(16))) char smem[NSLOT * SLOT];
  const int tid = threadIdx.x, lane = tid & 63;
  const int wid = __builtin_amdgcn_readfirstlane(tid >> 6);
  const int bid = blockIdx.x;
  const int b = bid >> 6, pair = bid & 63, br = pair >> 5, h = pair & 31;
  const char* xg  = (const char*)((br ? xB : xA) + (size_t)b * SQ * DM);
  const char* kg  = (const char*)((br ? kB : kA) + (size_t)(b * NH + h) * SK * DH);
  const char* vg  = (const char*)((br ? vB : vA) + (size_t)(b * NH + h) * SK * DH);
  const char* wqg = (const char*)((br ? wqB : wqA) + h * DH);
  const unsigned lbase = (unsigned)(uintptr_t)(lds_ptr)smem;
  const int qb = wid >> 1, kh = wid & 1;
  const int r32 = lane & 31, hh = lane >> 5;

  const unsigned lrow = lane >> 4;
  const unsigned fv0 = lrow * 256 + (((lane & 15) ^ (lrow << 2)) << 4);
  auto kvfill = [&](const char* src, unsigned slot) {
    const char* s0 = src + (wid - 4) * 8192;
    const unsigned dst = lbase + slot * SLOT + (wid - 4) * 8192;
    glds16x4_nt(s0, fv0, fv0 ^ 16, fv0 ^ 32, fv0 ^ 48, dst);
    glds16x4_nt(s0 + 4096, fv0, fv0 ^ 16, fv0 ^ 32, fv0 ^ 48, dst + 4096);
  };
  auto qfill = [&](int c) {
    const unsigned dst = lbase + (3 + (c & 1)) * SLOT;
#pragma unroll
    for (int i = 0; i < 2; ++i) {
      const int p = wid * 2 + i;
      const unsigned row = 4 * p + lrow;
      const unsigned ch = (lane & 15) ^ swz_b(row);
      glds16(wqg + (size_t)(32 * c + row) * (KO * 2) + ch * 16, dst + p * 1024);
      const unsigned xr = 16 * p + (lane >> 2);
      const unsigned xch = (lane & 3) ^ ((xr >> 2) & 3);
      const unsigned xrc = xr < SQ ? xr : SQ - 1;
      glds16(xg + xrc * (DM * 2) + 64 * c + xch * 16, dst + 8192 + p * 1024);
    }
  };

  if (wid >= 4) { kvfill(kg, 0); kvfill(vg, 1); kvfill(kg + SLOT, 2); }
  else { qfill(0); qfill(1); }

  f32x16 qacc[2] = {};
  {
    const unsigned blk = (lane >> 4) & 1, qq = (lane & 15) >> 2, pp = lane & 3;
    const unsigned pp2 = ((pp & 1) << 1) | (pp >> 1);
    const unsigned xrow = 32 * qb + r32;
    for (int c = 0; c < 16; ++c) {
      if (wid < 4) { if (c < 15) wait_vm<4>(); else wait_vm<0>(); }
      bar();
      const unsigned sl = lbase + (3 + (c & 1)) * SLOT;
#pragma unroll
      for (int s = 0; s < 2; ++s) {
        const bf16x8 xb = ldsr128(sl + 8192 + xrow * 64 + 16 * ((2 * s + hh) ^ ((xrow >> 2) & 3)));
#pragma unroll
        for (int dbi = 0; dbi < 2; ++dbi) {
          const unsigned db = 2 * kh + dbi;
          const unsigned cch = 4 * db + 2 * blk + (pp2 >> 1);
          const s16x4 lo = vtr(sl + off_b(16 * s + 8 * hh + qq, cch) + 8 * (pp2 & 1));
          const s16x4 hi = vtr(sl + off_b(16 * s + 8 * hh + 4 + qq, cch) + 8 * (pp2 & 1));
          qacc[dbi] = MFMA(PK8(lo, hi), xb, qacc[dbi]);
        }
      }
      bar();
      if (wid < 4 && c + 2 < 16) qfill(c + 2);
    }
  }
  bf16x8 qf[8];
  {
    u32x4 own[4];
#pragma unroll
    for (int dbi = 0; dbi < 2; ++dbi)
#pragma unroll
      for (int s = 0; s < 2; ++s) {
        const f32x16& a = qacc[dbi];
        own[2 * dbi + s] = (u32x4){cvtpk(a[8 * s + 0], a[8 * s + 1]), cvtpk(a[8 * s + 2], a[8 * s + 3]),
                                   cvtpk(a[8 * s + 4], a[8 * s + 5]), cvtpk(a[8 * s + 6], a[8 * s + 7])};
      }
    char* ex = smem + 3 * SLOT;
#pragma unroll
    for (int f = 0; f < 4; ++f) *(u32x4*)(ex + (wid * 4 + f) * 1024 + lane * 16) = own[f];
    bar();
    u32x4 oth[4];
#pragma unroll
    for (int f = 0; f < 4; ++f) oth[f] = *(const u32x4*)(ex + ((wid ^ 1) * 4 + f) * 1024 + lane * 16);
#pragma unroll
    for (int f = 0; f < 4; ++f) {
      qf[f]     = __builtin_bit_cast(bf16x8, kh ? oth[f] : own[f]);
      qf[4 + f] = __builtin_bit_cast(bf16x8, kh ? own[f] : oth[f]);
    }
  }

  constexpr float C = 0.08838834764831845f * 1.4426950408889634f;
  float m = -INFINITY, l = 0.f;
  f32x16 o[4] = {};
  bf16x8 pf[2];
  const unsigned krow = 32 * kh + r32;
  const unsigned blk = (lane >> 4) & 1, qq = (lane & 15) >> 2, pp = lane & 3;

  const unsigned kb0 = krow * 256 + 16 * (hh ^ swz_b(krow));
  const unsigned vrow = 32 * kh + 4 * hh + qq;
  const unsigned vb0 = vrow * 256 + 16 * ((2 * blk + (pp >> 1)) ^ swz_b(vrow)) + 8 * (pp & 1);
  const unsigned vb1 = (vrow + 8) * 256 + 16 * ((2 * blk + (pp >> 1)) ^ swz_b(vrow + 8)) + 8 * (pp & 1);
  unsigned cur = 0, prv = 4;
#pragma unroll 1
  for (int t = 0; t < 8; ++t) {
    if (wid >= 4) { if (t == 0) wait_vm<16>(); else if (t < 7) wait_vm<24>(); else wait_vm<8>(); }
    bar();
    if (wid >= 4) {
      if (t == 0) { kvfill(vg + SLOT, 3); kvfill(kg + 2 * SLOT, 4); }
      else if (t < 6) kvfill(kg + (t + 2) * SLOT, prv);
    }
    {
      const unsigned sl = lbase + cur * SLOT;
      f32x16 s = {};
#pragma unroll
      for (int ds = 0; ds < 8; ++ds) {
        const bf16x8 kf = ldsr128((kb0 ^ (32 * ds)) + sl);
        s = MFMA(kf, qf[ds], s);
      }
      float mx = s[0];
#pragma unroll
      for (int r = 1; r < 16; ++r) mx = fmaxf(mx, s[r]);
      mx = swap32_max(mx);
      const float mn = fmaxf(m, mx);
      const float alpha = __builtin_amdgcn_exp2f((m - mn) * C);
      m = mn;
      const float mc = -mn * C;
      float ps = 0.f;
#pragma unroll
      for (int r = 0; r < 16; ++r) { s[r] = __builtin_amdgcn_exp2f(fmaf(s[r], C, mc)); ps += s[r]; }
      l = l * alpha + ps;
#pragma unroll
      for (int d = 0; d < 4; ++d)
#pragma unroll
        for (int r = 0; r < 16; ++r) o[d][r] *= alpha;
#pragma unroll
      for (int ks = 0; ks < 2; ++ks) {
        u32x4 w = {cvtpk(s[8 * ks + 0], s[8 * ks + 1]), cvtpk(s[8 * ks + 2], s[8 * ks + 3]),
                   cvtpk(s[8 * ks + 4], s[8 * ks + 5]), cvtpk(s[8 * ks + 6], s[8 * ks + 7])};
        pf[ks] = __builtin_bit_cast(bf16x8, w);
      }
    }
    prv = cur; cur = (cur == 4) ? 0 : cur + 1;
    if (wid >= 4) { if (t < 6) wait_vm<24>(); else if (t == 6) wait_vm<16>(); else wait_vm<0>(); }
    bar();
    if (wid >= 4 && t < 6) kvfill(vg + (t + 2) * SLOT, prv);
    {
      const unsigned sl = lbase + cur * SLOT;
#pragma unroll
      for (int d = 0; d < 4; ++d)
#pragma unroll
        for (int ks = 0; ks < 2; ++ks) {
          const s16x4 lo = vtr((vb0 ^ (64 * d)) + sl + 4096 * ks);
          const s16x4 hi = vtr((vb1 ^ (64 * d)) + sl + 4096 * ks);
          o[d] = MFMA(PK8(lo, hi), pf[ks], o[d]);
        }
    }
    prv = cur; cur = (cur == 4) ? 0 : cur + 1;
  }

  l = swap32_sum(l);
  bar();
  float* mo = (float*)(smem + qb * SLOT);
  float* ml = (float*)(smem + 3 * SLOT) + qb * 128;
  if (kh == 1) {
#pragma unroll
    for (int d = 0; d < 4; ++d)
#pragma unroll
      for (int r = 0; r < 16; ++r) mo[(d * 16 + r) * 64 + lane] = o[d][r];
    ml[lane] = m; ml[64 + lane] = l;
  }
  bar();
  if (kh == 0) {
    const float m1 = ml[lane], l1 = ml[64 + lane];
    const float M = fmaxf(m, m1);
    const float a0 = __builtin_amdgcn_exp2f((m - M) * C), a1 = __builtin_amdgcn_exp2f((m1 - M) * C);
    const float inv = 1.f / (l * a0 + l1 * a1);
    const float f0 = a0 * inv, f1 = a1 * inv;
    u16* dst = ows + ((size_t)(br * MROWS + b * SQ + 32 * qb + r32)) * KO + h * DH + 4 * hh;
#pragma unroll
    for (int d = 0; d < 4; ++d)
#pragma unroll
      for (int g = 0; g < 4; ++g) {
        float v0 = o[d][4 * g + 0] * f0 + mo[(d * 16 + 4 * g + 0) * 64 + lane] * f1;
        float v1 = o[d][4 * g + 1] * f0 + mo[(d * 16 + 4 * g + 1) * 64 + lane] * f1;
        float v2 = o[d][4 * g + 2] * f0 + mo[(d * 16 + 4 * g + 2) * 64 + lane] * f1;
        float v3 = o[d][4 * g + 3] * f0 + mo[(d * 16 + 4 * g + 3) * 64 + lane] * f1;
        *(u32x2*)(dst + 32 * d + 8 * g) = (u32x2){cvtpk(v0, v1), cvtpk(v2, v3)};
      }
  }
}

constexpr int SPLITK = 4, KR = KO / SPLITK;
constexpr int GST = 12288 + 16384;
__global__ __launch_bounds__(256, 1) void oproj_kernel(const u16* __restrict__ ows, const u16* __restrict__ woA,
                                                       const u16* __restrict__ woB, float* __restrict__ part) {
  __shared__ __attribute__((aligned(16))) char smem[3 * GST];
  const int tid = threadIdx.x, lane = tid & 63;
  const int wid = __builtin_amdgcn_readfirstlane(tid >> 6);
  const int bid = blockIdx.x;
  const int mt = bid & 7, nt = (bid >> 3) & 3, br = (bid >> 5) & 1, sk = bid >> 6;
  const char* ag = (const char*)(ows + (size_t)(br * MROWS + mt * SQ) * KO + sk * KR);
  const char* bg = (const char*)((br ? woB : woA) + (size_t)sk * KR * DM + nt * 128);
  const unsigned lbase = (unsigned)(uintptr_t)(lds_ptr)smem;
  const int r32 = lane & 31, hh = lane >> 5;
  auto fill = [&](int c) {
    const unsigned dst = lbase + (c % 3) * GST;
#pragma unroll
    for (int i = 0; i < 3; ++i) {
      const int p = wid * 3 + i;
      const unsigned row = 8 * p + (lane >> 3);
      const unsigned ch = (lane & 7) ^ ((row >> 1) & 7);
      glds16(ag + (size_t)row * (KO * 2) + 128 * c + ch * 16, dst + p * 1024);
    }
#pragma unroll
    for (int i = 0; i < 4; ++i) {
      const int p = wid * 4 + i;
      const unsigned row = 4 * p + (lane >> 4);
      const unsigned ch = (lane & 15) ^ swz_b(row);
      glds16(bg + (size_t)(64 * c + row) * (DM * 2) + ch * 16, dst + 12288 + p * 1024);
    }
  };
  fill(0); fill(1);
  f32x16 acc[3] = {};
  const unsigned blk = (lane >> 4) & 1, qq = (lane & 15) >> 2, pp = lane & 3;
  for (int c = 0; c < 16; ++c) {
    if (c < 15) wait_vm<7>(); else wait_vm<0>();
    bar();
    if (c + 2 < 16) fill(c + 2);
    const unsigned sl = lbase + (c % 3) * GST;
#pragma unroll
    for (int ks = 0; ks < 4; ++ks) {
      const unsigned cch = 4 * wid + 2 * blk + (pp >> 1);
      const s16x4 lo = vtr(sl + 12288 + off_b(16 * ks + 8 * hh + qq, cch) + 8 * (pp & 1));
      const s16x4 hi = vtr(sl + 12288 + off_b(16 * ks + 8 * hh + 4 + qq, cch) + 8 * (pp & 1));
      const bf16x8 bf = PK8(lo, hi);
#pragma unroll
      for (int mb = 0; mb < 3; ++mb) {
        const unsigned row = 32 * mb + r32;
        const bf16x8 af = ldsr128(sl + row * 128 + 16 * ((2 * ks + hh) ^ ((row >> 1) & 7)));
        acc[mb] = MFMA(af, bf, acc[mb]);
      }
    }
  }
  float* dst = part + ((size_t)(sk * 2 + br) * MROWS + mt * SQ) * DM + nt * 128 + 32 * wid + r32;
#pragma unroll
  for (int mb = 0; mb < 3; ++mb)
#pragma unroll
    for (int r = 0; r < 16; ++r) dst[(size_t)(32 * mb + (r & 3) + 8 * (r >> 2) + 4 * hh) * DM] = acc[mb][r];
}

__global__ __launch_bounds__(256) void reduce_kernel(const float* __restrict__ part, u16* __restrict__ out) {
  const int i = blockIdx.x * 256 + threadIdx.x;
  const int br = i / (MROWS * DM / 4), j = i % (MROWS * DM / 4);
  const int mrow = j / (DM / 4), n4 = j % (DM / 4);
  f32x4 s = {};
#pragma unroll
  for (int sk = 0; sk < SPLITK; ++sk)
    s += *(const f32x4*)(part + ((size_t)(sk * 2 + br) * MROWS + mrow) * DM + n4 * 4);
  *(u32x2*)(out + (size_t)mrow * (2 * DM) + br * DM + n4 * 4) = (u32x2){cvtpk(s[0], s[1]), cvtpk(s[2], s[3])};
}

extern "C" void kernel_launch(void* const* d_in, const int* in_sizes, int n_in,
                              void* d_out, int out_size, void* d_ws, size_t ws_size,
                              hipStream_t stream) {
  const u16* xA = (const u16*)d_in[0]; const u16* kA = (const u16*)d_in[1]; const u16* vA = (const u16*)d_in[2];
  const u16* wqA = (const u16*)d_in[3]; const u16* woA = (const u16*)d_in[4];
  const u16* xB = (const u16*)d_in[5]; const u16* kB = (const u16*)d_in[6]; const u16* vB = (const u16*)d_in[7];
  const u16* wqB = (const u16*)d_in[8]; const u16* woB = (const u16*)d_in[9];
  u16* ows = (u16*)d_ws;
  float* part = (float*)((char*)d_ws + (size_t)2 * MROWS * KO * 2);
  attn_kernel<<<512, 384, 0, stream>>>(xA, kA, vA, wqA, xB, kB, vB, wqB, ows);
  oproj_kernel<<<256, 256, 0, stream>>>(ows, woA, woB, part);
  reduce_kernel<<<2 * MROWS * DM / 4 / 256, 256, 0, stream>>>(part, (u16*)d_out);
}
```

```cpp
#include <hip/hip_runtime.h>
#include <hip/hip_bf16.h>
#include <stdint.h>

typedef unsigned short u16;
using bf16x8 = __attribute__((ext_vector_type(8))) short;
using s16x4  = __attribute__((ext_vector_type(4))) short;
using f32x16 = __attribute__((ext_vector_type(16))) float;
using f32x4  = __attribute__((ext_vector_type(4))) float;
using u32x4  = __attribute__((ext_vector_type(4))) unsigned;
using u32x2  = __attribute__((ext_vector_type(2))) unsigned;
typedef short v4i16_t __attribute__((ext_vector_type(4)));
typedef __attribute__((address_space(3))) char* lds_ptr;

constexpr int NB = 8, SQ = 96, DM = 512, NH = 32, SK = 512, DH = 128;
constexpr int MROWS = NB * SQ;
constexpr int KO = NH * DH;

__device__ __forceinline__ unsigned swz_b(unsigned row) { return ((row & 3) << 2) | ((row >> 2) & 3); }
__device__ __forceinline__ unsigned off_b(unsigned row, unsigned ch) { return 256u * row + 16u * (ch ^ swz_b(row)); }

__device__ __forceinline__ void glds16(const void* g, unsigned lds_dst) {
  unsigned keep;
  asm volatile("s_mov_b32 %0, m0\n\ts_mov_b32 m0, %2\n\ts_nop 0\n\tglobal_load_lds_dwordx4 %1, off\n\ts_mov_b32 m0, %0"
               : "=&s"(keep) : "v"(g), "s"(lds_dst) : "memory");
}
__device__ __forceinline__ void glds16_nt(const void* g, unsigned lds_dst) {
  unsigned keep;
  asm volatile("s_mov_b32 %0, m0\n\ts_mov_b32 m0, %2\n\ts_nop 0\n\tglobal_load_lds_dwordx4 %1, off nt\n\ts_mov_b32 m0, %0"
               : "=&s"(keep) : "v"(g), "s"(lds_dst) : "memory");
}
__device__ __forceinline__ void glds16x4_nt(const void* sbase, unsigned v0, unsigned v1, unsigned v2, unsigned v3, unsigned lds_dst) {
  unsigned keep;
  asm volatile("s_mov_b32 %0, m0\n\ts_mov_b32 m0, %6\n\ts_nop 0\n\t"
               "global_load_lds_dwordx4 %1, %5 nt\n\t"
               "global_load_lds_dwordx4 %2, %5 offset:1024 nt\n\t"
               "global_load_lds_dwordx4 %3, %5 offset:2048 nt\n\t"
               "global_load_lds_dwordx4 %4, %5 offset:3072 nt\n\t"
               "s_mov_b32 m0, %0"
               : "=&s"(keep) : "v"(v0), "v"(v1), "v"(v2), "v"(v3), "s"(sbase), "s"(lds_dst) : "memory");
}
template <int N> __device__ __forceinline__ void wait_vm() { asm volatile("s_waitcnt vmcnt(%0)" :: "i"(N) : "memory"); }
__device__ __forceinline__ void bar() { asm volatile("s_waitcnt lgkmcnt(0)\n\ts_barrier" ::: "memory"); }

__device__ __forceinline__ s16x4 vtr(unsigned a) {
  return __builtin_bit_cast(s16x4, __builtin_amdgcn_ds_read_tr16_b64_v4i16((__attribute__((address_space(3))) v4i16_t*)(uintptr_t)a));
}
__device__ __forceinline__ bf16x8 ldsr128(unsigned a) {
  return *(const __attribute__((address_space(3))) bf16x8*)(uintptr_t)a;
}
typedef __bf16 bf16x2_t __attribute__((ext_vector_type(2)));
typedef float f32x2_t __attribute__((ext_vector_type(2)));
__device__ __forceinline__ unsigned cvtpk(float lo, float hi) {
  return __builtin_bit_cast(unsigned, __builtin_convertvector((f32x2_t){lo, hi}, bf16x2_t));
}
__device__ __forceinline__ float swap32_max(float v) {
  auto rr = __builtin_amdgcn_permlane32_swap(__float_as_uint(v), __float_as_uint(v), false, false);
  return fmaxf(__uint_as_float(rr[0]), __uint_as_float(rr[1]));
}
__device__ __forceinline__ float swap32_sum(float v) {
  auto rr = __builtin_amdgcn_permlane32_swap(__float_as_uint(v), __float_as_uint(v), false, false);
  return __uint_as_float(rr[0]) + __uint_as_float(rr[1]);
}
#define MFMA(a, b, c) __builtin_amdgcn_mfma_f32_32x32x16_bf16(a, b, c, 0, 0, 0)
#define PK8(L, H) (bf16x8){L[0], L[1], L[2], L[3], H[0], H[1], H[2], H[3]}

constexpr int SLOT = 16384, WQB = 6 * SLOT;
constexpr int NWQ = 5, XSB = WQB + NWQ * 8192;

__device__ __forceinline__ void glds16x4(const void* sbase, unsigned v0, unsigned v1, unsigned v2, unsigned v3, unsigned lds_dst) {
  unsigned keep;
  asm volatile("s_mov_b32 %0, m0\n\ts_mov_b32 m0, %6\n\ts_nop 0\n\t"
               "global_load_lds_dwordx4 %1, %5\n\t"
               "global_load_lds_dwordx4 %2, %5 offset:1024\n\t"
               "global_load_lds_dwordx4 %3, %5 offset:2048\n\t"
               "global_load_lds_dwordx4 %4, %5 offset:3072\n\t"
               "s_mov_b32 m0, %0"
               : "=&s"(keep) : "v"(v0), "v"(v1), "v"(v2), "v"(v3), "s"(sbase), "s"(lds_dst) : "memory");
}

__global__ __launch_bounds__(512, 2) void attn_kernel(
    const u16* __restrict__ xA, const u16* __restrict__ kA, const u16* __restrict__ vA, const u16* __restrict__ wqA,
    const u16* __restrict__ xB, const u16* __restrict__ kB, const u16* __restrict__ vB, const u16* __restrict__ wqB,
    u16* __restrict__ ows) {
  __shared__ __attribute__((aligned(16))) char smem[WQB + 65536];
  const int tid = threadIdx.x, lane = tid & 63;
  const int wid = __builtin_amdgcn_readfirstlane(tid >> 6);
  const int bid = blockIdx.x;
  const int xcd = bid & 7, rr = bid >> 3;
  const int br = xcd >> 2, h = (xcd & 3) * 8 + (rr >> 2), b0 = 2 * (rr & 3);
  const unsigned lbase = (unsigned)(uintptr_t)(lds_ptr)smem;
  const int r32 = lane & 31, hh = lane >> 5;

  if (wid >= 6) {
    const char* kg  = (const char*)((br ? kB : kA) + (size_t)(b0 * NH + h) * SK * DH);
    const char* vg  = (const char*)((br ? vB : vA) + (size_t)(b0 * NH + h) * SK * DH);
    const char* wqg = (const char*)((br ? wqB : wqA) + h * DH);
    constexpr size_t ITEM = (size_t)NH * SK * DH * 2;
    const unsigned lrow = lane >> 4;
    const unsigned fv0 = lrow * 256 + (((lane & 15) ^ (lrow << 2)) << 4);
    auto kvfill = [&](int f, int q0, int nq) {
      const int t = f >> 1;
      const char* s0 = ((f & 1) ? vg : kg) + (t >> 3) * ITEM + (t & 7) * SLOT;
      const unsigned dst = lbase + ((f & 1) * 3 + t % 3) * SLOT;
#pragma unroll
      for (int q = q0; q < q0 + nq; ++q) glds16x4_nt(s0 + q * 4096, fv0, fv0 ^ 16, fv0 ^ 32, fv0 ^ 48, dst + q * 4096);
    };
    if (wid == 6) {
      const unsigned w0 = lrow * (KO * 2) + (((lane & 15) ^ (lrow << 2)) << 4);
      const unsigned w1 = (w0 ^ 16) + 4 * (KO * 2) - 1024, w2 = (w0 ^ 32) + 8 * (KO * 2) - 2048, w3 = (w0 ^ 48) + 12 * (KO * 2) - 3072;
      auto wqfill = [&](int c) {
        const char* s0 = wqg + (size_t)c * (32 * KO * 2);
        const unsigned dst = lbase + WQB + (c % NWQ) * 8192;
        glds16x4(s0, w0, w1, w2, w3, dst);
        glds16x4(s0 + 16 * KO * 2, w0, w1, w2, w3, dst + 4096);
      };
#pragma unroll
      for (int c = 0; c < NWQ; ++c) wqfill(c);
      wait_vm<32>();
      bar();
#pragma unroll
      for (int c = 0; c < 16; ++c) {
        if (c < 11) wait_vm<24>(); else if (c == 11) wait_vm<40>(); else if (c == 12) wait_vm<32>();
        else if (c == 13) wait_vm<24>(); else if (c == 14) wait_vm<16>();
        bar();
        if (c + NWQ < 16) wqfill(c + NWQ);
        if (c == 10) kvfill(5, 0, 4);
      }
    } else {
      kvfill(0, 0, 4); kvfill(1, 0, 4);
      bar();
      kvfill(2, 0, 4);
#pragma unroll
      for (int c = 0; c < 16; ++c) {
        bar();
        if (c == 3) { wait_vm<47>(); kvfill(3, 0, 4); }
        if (c == 7) { wait_vm<47>(); kvfill(4, 0, 4); }
      }
    }
    bar(); bar();
    if (wid == 7) wait_vm<63>();
    bar();
    const int lq = 2 * (wid - 6);
#pragma unroll
    for (int T = 0; T < 15; ++T) {
      if (T == 0) { if (wid == 7) wait_vm<32>(); }
      else if (T == 1) { if (wid == 7) wait_vm<8>(); }
      else if (T < 14) wait_vm<16>(); else wait_vm<8>();
      bar();
      if (T > 0 && T < 14) kvfill(2 * T + 5, lq, 2);
      if (T < 13) kvfill(2 * T + 6, lq, 2);
      if (T == 7) bar();
    }
    wait_vm<0>();
    bar();
    bar();
    return;
  }

  const int qb = wid % 3, kh = wid / 3;
  const unsigned blk = (lane >> 4) & 1, qq = (lane & 15) >> 2, pp = lane & 3;
  bf16x8 qa[8];
  {
    const char* xp = (const char*)((br ? xB : xA) + (size_t)b0 * SQ * DM) + (size_t)(32 * wid + (lane >> 3)) * (DM * 2) + 16 * (lane & 7);
    u32x4 xr[8][4];
    auto xload = [&](int j) {
#pragma unroll
      for (int i = 0; i < 4; ++i) xr[j][i] = *(const u32x4*)(xp + i * (8 * DM * 2) + j * 128);
    };
#pragma unroll
    for (int j = 0; j < 4; ++j) xload(j);
    char* xs = smem + XSB + wid * 4096;
    const unsigned xw0 = (lane >> 3) * 128 + 16 * ((lane & 7) ^ ((lane >> 4) & 3));
    const unsigned xrd = r32 * 128 + 16 * (hh ^ ((r32 >> 1) & 7));
    f32x16 qacc[4] = {};
    bf16x8 xf[4], wf[2][8];
    auto xstage = [&](int j) {
#pragma unroll
      for (int i = 0; i < 4; ++i) *(u32x4*)(xs + i * 1024 + (xw0 ^ (64 * (i & 1)))) = xr[j][i];
      if (j + 4 < 8) xload(j + 4);
#pragma unroll
      for (int s = 0; s < 4; ++s) xf[s] = *(const bf16x8*)(xs + (xrd ^ (32 * s)));
    };
    const unsigned pp2 = ((pp & 1) << 1) | (pp >> 1);
    const unsigned a0 = lbase + WQB + off_b(8 * hh + qq, 2 * blk + (pp2 >> 1)) + 8 * (pp2 & 1);
    const unsigned a1 = lbase + WQB + off_b(8 * hh + 4 + qq, 2 * blk + (pp2 >> 1)) + 8 * (pp2 & 1);
    auto wread = [&](int c) {
#pragma unroll
      for (int s = 0; s < 2; ++s)
#pragma unroll
        for (int db = 0; db < 4; ++db) {
          const s16x4 lo = vtr((a0 ^ (64 * db)) + (c % NWQ) * 8192 + 4096 * s);
          const s16x4 hi = vtr((a1 ^ (64 * db)) + (c % NWQ) * 8192 + 4096 * s);
          wf[c & 1][4 * s + db] = PK8(lo, hi);
        }
    };
    xstage(0);
    bar();
    wread(0);
#pragma unroll
    for (int c = 0; c < 16; ++c) {
      bar();
      if (c + 1 < 16) wread(c + 1);
      __builtin_amdgcn_sched_barrier(0);
#pragma unroll
      for (int s = 0; s < 2; ++s)
#pragma unroll
        for (int db = 0; db < 4; ++db) qacc[db] = MFMA(wf[c & 1][4 * s + db], xf[2 * (c & 1) + s], qacc[db]);
      __builtin_amdgcn_sched_barrier(0);
      if ((c & 1) && c < 15) xstage((c + 1) / 2);
    }
    bar();
    char* ex = smem + WQB;
#pragma unroll
    for (int db = 0; db < 4; ++db)
#pragma unroll
      for (int s = 0; s < 2; ++s) {
        const f32x16& a = qacc[db];
        const u32x4 w = {cvtpk(a[8 * s + 0], a[8 * s + 1]), cvtpk(a[8 * s + 2], a[8 * s + 3]),
                         cvtpk(a[8 * s + 4], a[8 * s + 5]), cvtpk(a[8 * s + 6], a[8 * s + 7])};
        *(u32x4*)(ex + wid * 8192 + (2 * db + s) * 1024 + lane * 16) = w;
      }
    bar();
#pragma unroll
    for (int f = 0; f < 8; ++f) qa[f] = *(const bf16x8*)(ex + qb * 8192 + f * 1024 + lane * 16);
  }

  constexpr float C = 0.08838834764831845f * 1.4426950408889634f;
  constexpr float TAU = 8.0f / C;
  const unsigned krow = 32 * kh + r32;
  const unsigned kb0 = lbase + krow * 256 + 16 * (hh ^ swz_b(krow));
  const unsigned vrow = 32 * kh + 4 * hh + qq;
  const unsigned vb0 = lbase + 3 * SLOT + vrow * 256 + 16 * ((2 * blk + (pp >> 1)) ^ swz_b(vrow)) + 8 * (pp & 1);
  const unsigned vb1 = lbase + 3 * SLOT + (vrow + 8) * 256 + 16 * ((2 * blk + (pp >> 1)) ^ swz_b(vrow + 8)) + 8 * (pp & 1);

  float m = -INFINITY, l = 0.f;
  f32x16 o[4] = {};
  bf16x8 pf[2];

  auto readk = [&](bf16x8 (&kf)[8], int t) {
#pragma unroll
    for (int ds = 0; ds < 8; ++ds) kf[ds] = ldsr128((kb0 ^ (32 * ds)) + (t % 3) * SLOT);
  };
  auto readv = [&](bf16x8 (&vf)[8], int t) {
#pragma unroll
    for (int ks = 0; ks < 2; ++ks)
#pragma unroll
      for (int d = 0; d < 4; ++d) {
        const s16x4 lo = vtr((vb0 ^ (64 * d)) + (t % 3) * SLOT + 4096 * ks);
        const s16x4 hi = vtr((vb1 ^ (64 * d)) + (t % 3) * SLOT + 4096 * ks);
        vf[4 * ks + d] = PK8(lo, hi);
      }
  };
  auto pv = [&](const bf16x8 (&vf)[8]) {
#pragma unroll
    for (int ks = 0; ks < 2; ++ks)
#pragma unroll
      for (int d = 0; d < 4; ++d) o[d] = MFMA(vf[4 * ks + d], pf[ks], o[d]);
  };
  auto softmax = [&](f32x16& s, float& alpha) -> bool {
    float mx = fmaxf(fmaxf(fmaxf(s[0], s[1]), fmaxf(s[2], s[3])), fmaxf(fmaxf(s[4], s[5]), fmaxf(s[6], s[7])));
    mx = fmaxf(mx, fmaxf(fmaxf(fmaxf(s[8], s[9]), fmaxf(s[10], s[11])), fmaxf(fmaxf(s[12], s[13]), fmaxf(s[14], s[15]))));
    mx = swap32_max(mx);
    const bool need = __builtin_amdgcn_ballot_w64(mx > m + TAU) != 0;
    const float mn = need ? fmaxf(m, mx) : m;
    alpha = __builtin_amdgcn_exp2f((m - mn) * C);
    m = mn;
    const float mc = -mn * C;
#pragma unroll
    for (int r = 0; r < 16; ++r) s[r] = __builtin_amdgcn_exp2f(fmaf(s[r], C, mc));
    l = l * alpha + (((s[0] + s[1]) + (s[2] + s[3])) + ((s[4] + s[5]) + (s[6] + s[7])) +
                     (((s[8] + s[9]) + (s[10] + s[11])) + ((s[12] + s[13]) + (s[14] + s[15]))));
    asm volatile("" : "+v"(l));
#pragma unroll
    for (int ks = 0; ks < 2; ++ks) {
      u32x4 w = {cvtpk(s[8 * ks + 0], s[8 * ks + 1]), cvtpk(s[8 * ks + 2], s[8 * ks + 3]),
                 cvtpk(s[8 * ks + 4], s[8 * ks + 5]), cvtpk(s[8 * ks + 6], s[8 * ks + 7])};
      pf[ks] = __builtin_bit_cast(bf16x8, w);
    }
    return need;
  };
  auto finish = [&](const int it, const float mm, float ll) {
    char* ex = smem + WQB;
    ll = swap32_sum(ll);
#pragma unroll
    for (int dd = 0; dd < 2; ++dd)
#pragma unroll
      for (int g = 0; g < 4; ++g) {
        const f32x16& a = kh ? o[dd] : o[2 + dd];
        *(f32x4*)(ex + wid * 8192 + (dd * 4 + g) * 1024 + lane * 16) = (f32x4){a[4 * g], a[4 * g + 1], a[4 * g + 2], a[4 * g + 3]};
      }
    *(f32x2_t*)(ex + 49152 + wid * 512 + lane * 8) = (f32x2_t){mm, ll};
    bar();
    const int pw = wid < 3 ? wid + 3 : wid - 3;
    const f32x2_t ml = *(const f32x2_t*)(ex + 49152 + pw * 512 + lane * 8);
    const float M = fmaxf(mm, ml[0]);
    const float e0 = __builtin_amdgcn_exp2f((mm - M) * C), e1 = __builtin_amdgcn_exp2f((ml[0] - M) * C);
    const float inv = 1.f / (ll * e0 + ml[1] * e1);
    const float f0 = e0 * inv, f1 = e1 * inv;
    u16* dst = ows + ((size_t)(br * NH + h) * MROWS + (b0 + it) * SQ + 32 * qb + r32) * DH + 64 * kh + 8 * hh;
#pragma unroll
    for (int dd = 0; dd < 2; ++dd) {
      const f32x16& a = kh ? o[2 + dd] : o[dd];
      unsigned pk[4][2];
#pragma unroll
      for (int g = 0; g < 4; ++g) {
        const f32x4 p = *(const f32x4*)(ex + pw * 8192 + (dd * 4 + g) * 1024 + lane * 16);
        pk[g][0] = cvtpk(a[4 * g] * f0 + p[0] * f1, a[4 * g + 1] * f0 + p[1] * f1);
        pk[g][1] = cvtpk(a[4 * g + 2] * f0 + p[2] * f1, a[4 * g + 3] * f0 + p[3] * f1);
      }
#pragma unroll
      for (int gp = 0; gp < 2; ++gp) {
        auto sx = __builtin_amdgcn_permlane32_swap(pk[2 * gp][0], pk[2 * gp + 1][0], false, false);
        auto sy = __builtin_amdgcn_permlane32_swap(pk[2 * gp][1], pk[2 * gp + 1][1], false, false);
        *(u32x4*)(dst + 32 * dd + 16 * gp) = (u32x4){sx[0], sy[0], sx[1], sy[1]};
      }
    }
  };

  bar();
  {
    bf16x8 kf[8]; readk(kf, 0);
    f32x16 s = {};
#pragma unroll
    for (int ds = 0; ds < 8; ++ds) s = MFMA(kf[ds], qa[ds], s);
    float alpha; softmax(s, alpha);
  }
#pragma unroll
  for (int T = 0; T < 15; ++T) {
    bar();
    bf16x8 kf[8], vf[8];
    readk(kf, T + 1);
    __builtin_amdgcn_sched_barrier(0);
    readv(vf, T);
    __builtin_amdgcn_sched_barrier(0);
    f32x16 s = {};
#pragma unroll
    for (int ds = 0; ds < 8; ++ds) s = MFMA(kf[ds], qa[ds], s);
    if (T == 6) {
#pragma unroll
      for (int f = 0; f < 8; ++f) qa[f] = *(const bf16x8*)(smem + WQB + (qb + 3) * 8192 + f * 1024 + lane * 16);
    }
    __builtin_amdgcn_sched_barrier(0);
    const float mo = m, lo = l;
    if (T == 7) { m = -INFINITY; l = 0.f; }
    pv(vf);
    float alpha;
    const bool need = softmax(s, alpha);
#pragma unroll
    for (int i = 0; i < 8; ++i) {
      __builtin_amdgcn_sched_group_barrier(0x008, 1, 0);
      __builtin_amdgcn_sched_group_barrier(0x002, 12, 0);
    }
    __builtin_amdgcn_sched_barrier(0);
    if (T == 7) {
      finish(0, mo, lo);
#pragma unroll
      for (int d = 0; d < 4; ++d) o[d] = (f32x16){};
    } else if (need) {
#pragma unroll
      for (int d = 0; d < 4; ++d)
#pragma unroll
        for (int r = 0; r < 16; ++r) o[d][r] *= alpha;
    }
  }
  bar();
  { bf16x8 vf[8]; readv(vf, 15); pv(vf); }
  finish(1, m, l);
}

constexpr int SPLITK = 4, KR = KO / SPLITK;
constexpr int GST = 12288 + 16384, GNS = 5;
__global__ __launch_bounds__(512, 2) void oproj_kernel(const u16* __restrict__ ows, const u16* __restrict__ woA,
                                                       const u16* __restrict__ woB, float* __restrict__ part) {
  __shared__ __attribute__((aligned(16))) char smem[GNS * GST];
  const int tid = threadIdx.x, lane = tid & 63;
  const int wid = __builtin_amdgcn_readfirstlane(tid >> 6);
  const int bid = blockIdx.x;
  const int xcd = bid & 7, rr = bid >> 3;
  const int br = xcd >> 2, sk = xcd & 3, mt = rr & 7, nt = rr >> 3;
  const int wn = wid & 3, wk = wid >> 2;
  const unsigned lbase = (unsigned)(uintptr_t)(lds_ptr)smem;
  const int r32 = lane & 31, hh = lane >> 5;
  const char* gsrc; unsigned v0, v1, v2, v3, ldst;
  if (wid < 4) {
    const unsigned lrow = lane >> 4;
    gsrc = (const char*)((br ? woB : woA) + ((size_t)sk * KR + 16 * wid) * DM + nt * 128);
    v0 = lrow * (DM * 2) + (((lane & 15) ^ (lrow << 2)) << 4);
    v1 = (v0 ^ 16) + 4 * (DM * 2) - 1024; v2 = (v0 ^ 32) + 8 * (DM * 2) - 2048; v3 = (v0 ^ 48) + 12 * (DM * 2) - 3072;
    ldst = lbase + 12288 + wid * 4096;
  } else {
    const unsigned lrow = lane >> 3, p0 = 3 * (wid - 4);
    gsrc = (const char*)(ows + ((size_t)(br * NH + sk * (KR / DH)) * MROWS + mt * SQ + 8 * p0) * DH);
    auto vo = [&](unsigned p) { return (8 * p + lrow) * (DH * 2) + (((lane & 7) ^ ((4 * (p0 + p) + (lrow >> 1)) & 7)) << 4) - 1024 * p; };
    v0 = vo(0); v1 = vo(1); v2 = vo(2); v3 = 0;
    ldst = lbase + p0 * 1024;
  }
  auto fill = [&](int c) {
    const unsigned dst = ldst + (c % GNS) * GST;
    if (wid < 4) glds16x4(gsrc + (size_t)c * (64 * DM * 2), v0, v1, v2, v3, dst);
    else {
      const char* s0 = gsrc + (size_t)(c >> 1) * (MROWS * DH * 2) + 128 * (c & 1);
      unsigned keep;
      asm volatile("s_mov_b32 %0, m0\n\ts_mov_b32 m0, %5\n\ts_nop 0\n\t"
                   "global_load_lds_dwordx4 %1, %4\n\t"
                   "global_load_lds_dwordx4 %2, %4 offset:1024\n\t"
                   "global_load_lds_dwordx4 %3, %4 offset:2048\n\t"
                   "s_mov_b32 m0, %0"
                   : "=&s"(keep) : "v"(v0), "v"(v1), "v"(v2), "s"(s0), "s"(dst) : "memory");
    }
  };
  auto waitst = [&](int n) {
    if (wid < 4) { if (n == 3) wait_vm<12>(); else if (n == 2) wait_vm<8>(); else if (n == 1) wait_vm<4>(); else wait_vm<0>(); }
    else { if (n == 3) wait_vm<9>(); else if (n == 2) wait_vm<6>(); else if (n == 1) wait_vm<3>(); else wait_vm<0>(); }
  };
  const unsigned blk = (lane >> 4) & 1, qq = (lane & 15) >> 2, pp = lane & 3;
  const unsigned cch = 4 * wn + 2 * blk + (pp >> 1);
  const unsigned b0a = lbase + 12288 + off_b(32 * wk + 8 * hh + qq, cch) + 8 * (pp & 1);
  const unsigned b1a = lbase + 12288 + off_b(32 * wk + 8 * hh + 4 + qq, cch) + 8 * (pp & 1);
  const unsigned aa = lbase + r32 * 128 + 16 * ((4 * wk + hh) ^ ((r32 >> 1) & 7));
  bf16x8 wf[2][2], af[2][6];
  auto rd = [&](int c) {
    const unsigned so = (c % GNS) * GST;
#pragma unroll
    for (int ks = 0; ks < 2; ++ks) {
      const s16x4 lo = vtr(b0a + so + 4096 * ks), hi = vtr(b1a + so + 4096 * ks);
      wf[c & 1][ks] = PK8(lo, hi);
#pragma unroll
      for (int mb = 0; mb < 3; ++mb) af[c & 1][3 * ks + mb] = ldsr128((aa ^ (32 * ks)) + so + 4096 * mb);
    }
  };
#pragma unroll
  for (int c = 0; c < GNS; ++c) fill(c);
  f32x16 acc[3] = {};
  if (wid < 4) wait_vm<16>(); else wait_vm<12>();
  bar();
  rd(0);
#pragma unroll
  for (int c = 0; c < 16; ++c) {
    if (c < 15) {
      waitst(c + 4 < 16 ? 3 : 14 - c);
      bar();
      if (c + GNS < 16) fill(c + GNS);
      rd(c + 1);
    }
#pragma unroll
    for (int ks = 0; ks < 2; ++ks)
#pragma unroll
      for (int mb = 0; mb < 3; ++mb) acc[mb] = MFMA(wf[c & 1][ks], af[c & 1][3 * ks + mb], acc[mb]);
    if (c < 15) {
#pragma unroll
      for (int i = 0; i < 5; ++i) {
        __builtin_amdgcn_sched_group_barrier(0x008, 1, 0);
        __builtin_amdgcn_sched_group_barrier(0x100, 2, 0);
      }
      __builtin_amdgcn_sched_group_barrier(0x008, 1, 0);
    }
    __builtin_amdgcn_sched_barrier(0);
  }
  bar();
  char* ex = smem + wn * 12288 + lane * 16;
  if (wk) {
#pragma unroll
    for (int mb = 0; mb < 3; ++mb)
#pragma unroll
      for (int g = 0; g < 4; ++g)
        *(f32x4*)(ex + (mb * 4 + g) * 1024) = (f32x4){acc[mb][4 * g], acc[mb][4 * g + 1], acc[mb][4 * g + 2], acc[mb][4 * g + 3]};
  }
  bar();
  if (!wk) {
    float* dst = part + ((((size_t)sk * 2 + br) * 32 + mt * 4 + nt) * 4 + wn) * 3072 + lane * 4;
#pragma unroll
    for (int mb = 0; mb < 3; ++mb)
#pragma unroll
      for (int g = 0; g < 4; ++g) {
        const f32x4 p = *(const f32x4*)(ex + (mb * 4 + g) * 1024);
        *(f32x4*)(dst + (mb * 4 + g) * 256) =
            (f32x4){acc[mb][4 * g] + p[0], acc[mb][4 * g + 1] + p[1], acc[mb][4 * g + 2] + p[2], acc[mb][4 * g + 3] + p[3]};
      }
  }
}

__global__ __launch_bounds__(256) void reduce_kernel(const float* __restrict__ part, u16* __restrict__ out) {
  const int i = blockIdx.x * 256 + threadIdx.x;
  const int r32 = i & 31, q = (i >> 5) % 12, tw = (i >> 5) / 12;
  const int wn = tw & 3, tile = tw >> 2;
  const int nt = tile & 3, mt = (tile >> 2) & 7, br = tile >> 5;
  const float* src = part + ((size_t)tw * 12 + q) * 256 + r32 * 4;
  f32x4 s0 = {}, s1 = {};
#pragma unroll
  for (int sk = 0; sk < SPLITK; ++sk) {
    s0 += *(const f32x4*)(src + (size_t)sk * (2 * MROWS * DM));
    s1 += *(const f32x4*)(src + (size_t)sk * (2 * MROWS * DM) + 128);
  }
  const int row = mt * SQ + 32 * (q >> 2) + r32, col = br * DM + nt * 128 + wn * 32 + 8 * (q & 3);
  *(u32x4*)(out + (size_t)row * (2 * DM) + col) = (u32x4){cvtpk(s0[0], s0[1]), cvtpk(s0[2], s0[3]), cvtpk(s1[0], s1[1]), cvtpk(s1[2], s1[3])};
}

extern "C" void kernel_launch(void* const* d_in, const int* in_sizes, int n_in,
                              void* d_out, int out_size, void* d_ws, size_t ws_size,
                              hipStream_t stream) {
  const u16* xA = (const u16*)d_in[0]; const u16* kA = (const u16*)d_in[1]; const u16* vA = (const u16*)d_in[2];
  const u16* wqA = (const u16*)d_in[3]; const u16* woA = (const u16*)d_in[4];
  const u16* xB = (const u16*)d_in[5]; const u16* kB = (const u16*)d_in[6]; const u16* vB = (const u16*)d_in[7];
  const u16* wqB = (const u16*)d_in[8]; const u16* woB = (const u16*)d_in[9];
  u16* ows = (u16*)d_ws;
  float* part = (float*)((char*)d_ws + (size_t)2 * MROWS * KO * 2);
  attn_kernel<<<256, 512, 0, stream>>>(xA, kA, vA, wqA, xB, kB, vB, wqB, ows);
  oproj_kernel<<<256, 512, 0, stream>>>(ows, woA, woB, part);
  reduce_kernel<<<2 * MROWS * DM / 8 / 256, 256, 0, stream>>>(part, (u16*)d_out);
}
```

```cpp
#include <hip/hip_runtime.h>
#include <hip/hip_bf16.h>
#include <stdint.h>

typedef unsigned short u16;
using bf16x8 = __attribute__((ext_vector_type(8))) short;
using s16x4  = __attribute__((ext_vector_type(4))) short;
using f32x16 = __attribute__((ext_vector_type(16))) float;
using f32x4  = __attribute__((ext_vector_type(4))) float;
using u32x4  = __attribute__((ext_vector_type(4))) unsigned;
using u32x2  = __attribute__((ext_vector_type(2))) unsigned;
typedef short v4i16_t __attribute__((ext_vector_type(4)));
typedef __attribute__((address_space(3))) char* lds_ptr;

constexpr int NB = 8, SQ = 96, DM = 512, NH = 32, SK = 512, DH = 128;
constexpr int MROWS = NB * SQ;
constexpr int KO = NH * DH;

__device__ __forceinline__ unsigned swz_b(unsigned row) { return ((row & 3) << 2) | ((row >> 2) & 3); }
__device__ __forceinline__ unsigned off_b(unsigned row, unsigned ch) { return 256u * row + 16u * (ch ^ swz_b(row)); }

__device__ __forceinline__ void glds16(const void* g, unsigned lds_dst) {
  unsigned keep;
  asm volatile("s_mov_b32 %0, m0\n\ts_mov_b32 m0, %2\n\ts_nop 0\n\tglobal_load_lds_dwordx4 %1, off\n\ts_mov_b32 m0, %0"
               : "=&s"(keep) : "v"(g), "s"(lds_dst) : "memory");
}
__device__ __forceinline__ void glds16_nt(const void* g, unsigned lds_dst) {
  unsigned keep;
  asm volatile("s_mov_b32 %0, m0\n\ts_mov_b32 m0, %2\n\ts_nop 0\n\tglobal_load_lds_dwordx4 %1, off nt\n\ts_mov_b32 m0, %0"
               : "=&s"(keep) : "v"(g), "s"(lds_dst) : "memory");
}
__device__ __forceinline__ void glds16x4_nt(const void* sbase, unsigned v0, unsigned v1, unsigned v2, unsigned v3, unsigned lds_dst) {
  unsigned keep;
  asm volatile("s_mov_b32 %0, m0\n\ts_mov_b32 m0, %6\n\ts_nop 0\n\t"
               "global_load_lds_dwordx4 %1, %5 nt\n\t"
               "global_load_lds_dwordx4 %2, %5 offset:1024 nt\n\t"
               "global_load_lds_dwordx4 %3, %5 offset:2048 nt\n\t"
               "global_load_lds_dwordx4 %4, %5 offset:3072 nt\n\t"
               "s_mov_b32 m0, %0"
               : "=&s"(keep) : "v"(v0), "v"(v1), "v"(v2), "v"(v3), "s"(sbase), "s"(lds_dst) : "memory");
}
template <int N> __device__ __forceinline__ void wait_vm() { asm volatile("s_waitcnt vmcnt(%0)" :: "i"(N) : "memory"); }
__device__ __forceinline__ void bar() { asm volatile("s_waitcnt lgkmcnt(0)\n\ts_barrier" ::: "memory"); }

__device__ __forceinline__ s16x4 vtr(unsigned a) {
  return __builtin_bit_cast(s16x4, __builtin_amdgcn_ds_read_tr16_b64_v4i16((__attribute__((address_space(3))) v4i16_t*)(uintptr_t)a));
}
__device__ __forceinline__ bf16x8 ldsr128(unsigned a) {
  return *(const __attribute__((address_space(3))) bf16x8*)(uintptr_t)a;
}
typedef __bf16 bf16x2_t __attribute__((ext_vector_type(2)));
typedef float f32x2_t __attribute__((ext_vector_type(2)));
__device__ __forceinline__ unsigned cvtpk(float lo, float hi) {
  return __builtin_bit_cast(unsigned, __builtin_convertvector((f32x2_t){lo, hi}, bf16x2_t));
}
__device__ __forceinline__ float swap32_max(float v) {
  auto rr = __builtin_amdgcn_permlane32_swap(__float_as_uint(v), __float_as_uint(v), false, false);
  return fmaxf(__uint_as_float(rr[0]), __uint_as_float(rr[1]));
}
__device__ __forceinline__ float swap32_sum(float v) {
  auto rr = __builtin_amdgcn_permlane32_swap(__float_as_uint(v), __float_as_uint(v), false, false);
  return __uint_as_float(rr[0]) + __uint_as_float(rr[1]);
}
#define MFMA(a, b, c) __builtin_amdgcn_mfma_f32_32x32x16_bf16(a, b, c, 0, 0, 0)
#define PK8(L, H) (bf16x8){L[0], L[1], L[2], L[3], H[0], H[1], H[2], H[3]}

constexpr int SLOT = 16384, WQB = 6 * SLOT;
constexpr int NWQ = 5, XSB = WQB + NWQ * 8192;

__device__ __forceinline__ void glds16x4(const void* sbase, unsigned v0, unsigned v1, unsigned v2, unsigned v3, unsigned lds_dst) {
  unsigned keep;
  asm volatile("s_mov_b32 %0, m0\n\ts_mov_b32 m0, %6\n\ts_nop 0\n\t"
               "global_load_lds_dwordx4 %1, %5\n\t"
               "global_load_lds_dwordx4 %2, %5 offset:1024\n\t"
               "global_load_lds_dwordx4 %3, %5 offset:2048\n\t"
               "global_load_lds_dwordx4 %4, %5 offset:3072\n\t"
               "s_mov_b32 m0, %0"
               : "=&s"(keep) : "v"(v0), "v"(v1), "v"(v2), "v"(v3), "s"(sbase), "s"(lds_dst) : "memory");
}

__global__ __launch_bounds__(512, 2) void attn_kernel(
    const u16* __restrict__ xA, const u16* __restrict__ kA, const u16* __restrict__ vA, const u16* __restrict__ wqA,
    const u16* __restrict__ xB, const u16* __restrict__ kB, const u16* __restrict__ vB, const u16* __restrict__ wqB,
    u16* __restrict__ ows) {
  __shared__ __attribute__((aligned(16))) char smem[WQB + 65536];
  const int tid = threadIdx.x, lane = tid & 63;
  const int wid = __builtin_amdgcn_readfirstlane(tid >> 6);
  const int bid = blockIdx.x;
  const int xcd = bid & 7, rr = bid >> 3;
  const int br = xcd >> 2, h = (xcd & 3) * 8 + (rr >> 2), b0 = 2 * (rr & 3);
  const unsigned lbase = (unsigned)(uintptr_t)(lds_ptr)smem;
  const int r32 = lane & 31, hh = lane >> 5;

  if (wid >= 6) {
    const char* kg  = (const char*)((br ? kB : kA) + (size_t)(b0 * NH + h) * SK * DH);
    const char* vg  = (const char*)((br ? vB : vA) + (size_t)(b0 * NH + h) * SK * DH);
    const char* wqg = (const char*)((br ? wqB : wqA) + h * DH);
    constexpr size_t ITEM = (size_t)NH * SK * DH * 2;
    const unsigned lrow = lane >> 4;
    const unsigned fv0 = lrow * 256 + (((lane & 15) ^ (lrow << 2)) << 4);
    auto kvfill = [&](int f, int q0, int nq) {
      const int t = f >> 1;
      const char* s0 = ((f & 1) ? vg : kg) + (t >> 3) * ITEM + (t & 7) * SLOT;
      const unsigned dst = lbase + ((f & 1) * 3 + t % 3) * SLOT;
#pragma unroll
      for (int q = q0; q < q0 + nq; ++q) glds16x4_nt(s0 + q * 4096, fv0, fv0 ^ 16, fv0 ^ 32, fv0 ^ 48, dst + q * 4096);
    };
    u32x4 rb[3][16];
    auto rload = [&](int f) {
      const int t = f >> 1;
      const char* s0 = ((f & 1) ? vg : kg) + (t >> 3) * ITEM + (t & 7) * SLOT;
#pragma unroll
      for (int k = 0; k < 16; ++k)
        rb[((f - 6) >> 1) % 3][k] = __builtin_nontemporal_load((const u32x4*)(s0 + k * 1024 + (fv0 ^ (16 * (k & 3)))));
    };
    auto rstore = [&](int f) {
      char* dst = smem + ((f & 1) * 3 + (f >> 1) % 3) * SLOT + lane * 16;
#pragma unroll
      for (int k = 0; k < 16; ++k) *(u32x4*)(dst + k * 1024) = rb[((f - 6) >> 1) % 3][k];
    };
    if (wid == 6) {
      const unsigned w0 = lrow * (KO * 2) + (((lane & 15) ^ (lrow << 2)) << 4);
      const unsigned w1 = (w0 ^ 16) + 4 * (KO * 2) - 1024, w2 = (w0 ^ 32) + 8 * (KO * 2) - 2048, w3 = (w0 ^ 48) + 12 * (KO * 2) - 3072;
      auto wqfill = [&](int c) {
        const char* s0 = wqg + (size_t)c * (32 * KO * 2);
        const unsigned dst = lbase + WQB + (c % NWQ) * 8192;
        glds16x4(s0, w0, w1, w2, w3, dst);
        glds16x4(s0 + 16 * KO * 2, w0, w1, w2, w3, dst + 4096);
      };
#pragma unroll
      for (int c = 0; c < NWQ; ++c) wqfill(c);
      wait_vm<32>();
      bar();
#pragma unroll
      for (int c = 0; c < 16; ++c) {
        if (c < 11) wait_vm<24>(); else if (c == 11) wait_vm<40>(); else if (c == 12) wait_vm<32>();
        else if (c == 13) wait_vm<40>(); else if (c == 14) wait_vm<32>();
        bar();
        if (c + NWQ < 16) wqfill(c + NWQ);
        if (c == 10) rload(7);
        if (c == 12) rload(9);
        if (c == 14) rload(11);
      }
      bar(); bar();
      bar();
#pragma unroll
      for (int T = 0; T < 15; ++T) {
        bar();
        if (T > 0 && T < 11) { rstore(2 * T + 5); rload(2 * T + 11); }
        else if (T >= 12) rstore(2 * T + 3);
        if (T == 7) bar();
      }
    } else {
      kvfill(0, 0, 4); kvfill(2, 0, 4);
      bar();
      kvfill(1, 0, 4);
#pragma unroll
      for (int c = 0; c < 16; ++c) {
        bar();
        if (c == 3) { wait_vm<47>(); kvfill(4, 0, 4); }
        if (c == 5) { wait_vm<47>(); kvfill(3, 0, 4); }
        if (c == 7) { wait_vm<47>(); kvfill(5, 0, 4); }
        if (c == 9) { wait_vm<47>(); rload(6); }
        if (c == 11) { wait_vm<47>(); rload(8); }
        if (c == 13) { wait_vm<47>(); rload(10); }
      }
      bar(); bar();
      bar();
#pragma unroll
      for (int T = 0; T < 15; ++T) {
        bar();
        if (T == 0) { rstore(6); rload(12); }
        if (T < 9) { rstore(2 * T + 8); rload(2 * T + 14); }
        else if (T >= 10 && T < 13) rstore(2 * T + 6);
        if (T == 7) bar();
      }
    }
    bar();
    bar();
    return;
  }

  const int qb = wid % 3, kh = wid / 3;
  const unsigned blk = (lane >> 4) & 1, qq = (lane & 15) >> 2, pp = lane & 3;
  bf16x8 qa[8];
  {
    const char* xp = (const char*)((br ? xB : xA) + (size_t)b0 * SQ * DM) + (size_t)(32 * wid + (lane >> 3)) * (DM * 2) + 16 * (lane & 7);
    u32x4 xr[8][4];
    auto xload = [&](int j) {
#pragma unroll
      for (int i = 0; i < 4; ++i) xr[j][i] = *(const u32x4*)(xp + i * (8 * DM * 2) + j * 128);
    };
#pragma unroll
    for (int j = 0; j < 4; ++j) xload(j);
    char* xs = smem + XSB + wid * 4096;
    const unsigned xw0 = (lane >> 3) * 128 + 16 * ((lane & 7) ^ ((lane >> 4) & 3));
    const unsigned xrd = r32 * 128 + 16 * (hh ^ ((r32 >> 1) & 7));
    f32x16 qacc[4] = {};
    bf16x8 xf[2][4], wf[2][8];
    auto xstage = [&](int j) {
#pragma unroll
      for (int i = 0; i < 4; ++i) *(u32x4*)(xs + i * 1024 + (xw0 ^ (64 * (i & 1)))) = xr[j][i];
      if (j + 4 < 8) xload(j + 4);
#pragma unroll
      for (int s = 0; s < 4; ++s) xf[j & 1][s] = *(const bf16x8*)(xs + (xrd ^ (32 * s)));
    };
    const unsigned pp2 = ((pp & 1) << 1) | (pp >> 1);
    const unsigned a0 = lbase + WQB + off_b(8 * hh + qq, 2 * blk + (pp2 >> 1)) + 8 * (pp2 & 1);
    const unsigned a1 = lbase + WQB + off_b(8 * hh + 4 + qq, 2 * blk + (pp2 >> 1)) + 8 * (pp2 & 1);
    auto wread = [&](int c) {
#pragma unroll
      for (int s = 0; s < 2; ++s)
#pragma unroll
        for (int db = 0; db < 4; ++db) {
          const s16x4 lo = vtr((a0 ^ (64 * db)) + (c % NWQ) * 8192 + 4096 * s);
          const s16x4 hi = vtr((a1 ^ (64 * db)) + (c % NWQ) * 8192 + 4096 * s);
          wf[c & 1][4 * s + db] = PK8(lo, hi);
        }
    };
    xstage(0);
    bar();
    wread(0);
#pragma unroll
    for (int c = 0; c < 16; ++c) {
      bar();
      if (c + 1 < 16) wread(c + 1);
      if ((c & 1) && c < 15) xstage((c + 1) / 2);
      __builtin_amdgcn_sched_barrier(0);
#pragma unroll
      for (int s = 0; s < 2; ++s)
#pragma unroll
        for (int db = 0; db < 4; ++db) qacc[db] = MFMA(wf[c & 1][4 * s + db], xf[(c >> 1) & 1][2 * (c & 1) + s], qacc[db]);
      __builtin_amdgcn_sched_barrier(0);
    }
    bar();
    char* ex = smem + WQB;
#pragma unroll
    for (int db = 0; db < 4; ++db)
#pragma unroll
      for (int s = 0; s < 2; ++s) {
        const f32x16& a = qacc[db];
        const u32x4 w = {cvtpk(a[8 * s + 0], a[8 * s + 1]), cvtpk(a[8 * s + 2], a[8 * s + 3]),
                         cvtpk(a[8 * s + 4], a[8 * s + 5]), cvtpk(a[8 * s + 6], a[8 * s + 7])};
        *(u32x4*)(ex + wid * 8192 + (2 * db + s) * 1024 + lane * 16) = w;
      }
    bar();
#pragma unroll
    for (int f = 0; f < 8; ++f) qa[f] = *(const bf16x8*)(ex + qb * 8192 + f * 1024 + lane * 16);
  }

  constexpr float C = 0.08838834764831845f * 1.4426950408889634f;
  constexpr float TAU = 8.0f / C;
  const unsigned krow = 32 * kh + r32;
  const unsigned kb0 = lbase + krow * 256 + 16 * (hh ^ swz_b(krow));
  const unsigned vrow = 32 * kh + 4 * hh + qq;
  const unsigned vb0 = lbase + 3 * SLOT + vrow * 256 + 16 * ((2 * blk + (pp >> 1)) ^ swz_b(vrow)) + 8 * (pp & 1);
  const unsigned vb1 = lbase + 3 * SLOT + (vrow + 8) * 256 + 16 * ((2 * blk + (pp >> 1)) ^ swz_b(vrow + 8)) + 8 * (pp & 1);

  float m = -INFINITY, l = 0.f;
  f32x16 o[4] = {};
  bf16x8 pf[2];

  auto readk = [&](bf16x8 (&kf)[8], int t) {
#pragma unroll
    for (int ds = 0; ds < 8; ++ds) kf[ds] = ldsr128((kb0 ^ (32 * ds)) + (t % 3) * SLOT);
  };
  auto readv = [&](bf16x8 (&vf)[8], int t) {
#pragma unroll
    for (int ks = 0; ks < 2; ++ks)
#pragma unroll
      for (int d = 0; d < 4; ++d) {
        const s16x4 lo = vtr((vb0 ^ (64 * d)) + (t % 3) * SLOT + 4096 * ks);
        const s16x4 hi = vtr((vb1 ^ (64 * d)) + (t % 3) * SLOT + 4096 * ks);
        vf[4 * ks + d] = PK8(lo, hi);
      }
  };
  auto pv = [&](const bf16x8 (&vf)[8]) {
#pragma unroll
    for (int ks = 0; ks < 2; ++ks)
#pragma unroll
      for (int d = 0; d < 4; ++d) o[d] = MFMA(vf[4 * ks + d], pf[ks], o[d]);
  };
  auto softmax = [&](f32x16& s, float& alpha) -> bool {
#define MX3(a, b, c) __builtin_elementwise_maximum(__builtin_elementwise_maximum(a, b), c)
    float mx = MX3(MX3(MX3(s[0], s[1], s[2]), MX3(s[3], s[4], s[5]), MX3(s[6], s[7], s[8])),
                   MX3(MX3(s[9], s[10], s[11]), MX3(s[12], s[13], s[14]), s[15]), m);
    {
      auto rr = __builtin_amdgcn_permlane32_swap(__float_as_uint(mx), __float_as_uint(mx), false, false);
      mx = __builtin_elementwise_maximum(__uint_as_float(rr[0]), __uint_as_float(rr[1]));
    }
    const bool need = __builtin_amdgcn_ballot_w64(!(mx <= m + TAU)) != 0;
    const float mn = need ? mx : m;
    alpha = __builtin_amdgcn_exp2f((m - mn) * C);
    m = mn;
    const float mc = -mn * C;
    f32x2_t ps[4];
#pragma unroll
    for (int r = 0; r < 8; ++r) {
      const f32x2_t t = __builtin_elementwise_fma((f32x2_t){s[2 * r], s[2 * r + 1]}, (f32x2_t){C, C}, (f32x2_t){mc, mc});
      s[2 * r] = __builtin_amdgcn_exp2f(t[0]); s[2 * r + 1] = __builtin_amdgcn_exp2f(t[1]);
      if (r < 4) ps[r] = (f32x2_t){s[2 * r], s[2 * r + 1]}; else ps[r - 4] += (f32x2_t){s[2 * r], s[2 * r + 1]};
    }
    const f32x2_t pt = (ps[0] + ps[1]) + (ps[2] + ps[3]);
    l = l * alpha + (pt[0] + pt[1]);
    asm volatile("" : "+v"(l));
#pragma unroll
    for (int ks = 0; ks < 2; ++ks) {
      u32x4 w = {cvtpk(s[8 * ks + 0], s[8 * ks + 1]), cvtpk(s[8 * ks + 2], s[8 * ks + 3]),
                 cvtpk(s[8 * ks + 4], s[8 * ks + 5]), cvtpk(s[8 * ks + 6], s[8 * ks + 7])};
      pf[ks] = __builtin_bit_cast(bf16x8, w);
    }
    return need;
  };
  auto finish = [&](const int it, const float mm, float ll) {
    char* ex = smem + WQB;
    ll = swap32_sum(ll);
#pragma unroll
    for (int dd = 0; dd < 2; ++dd)
#pragma unroll
      for (int g = 0; g < 4; ++g) {
        const f32x16& a = kh ? o[dd] : o[2 + dd];
        *(f32x4*)(ex + wid * 8192 + (dd * 4 + g) * 1024 + lane * 16) = (f32x4){a[4 * g], a[4 * g + 1], a[4 * g + 2], a[4 * g + 3]};
      }
    *(f32x2_t*)(ex + 49152 + wid * 512 + lane * 8) = (f32x2_t){mm, ll};
    bar();
    const int pw = wid < 3 ? wid + 3 : wid - 3;
    const f32x2_t ml = *(const f32x2_t*)(ex + 49152 + pw * 512 + lane * 8);
    const float M = fmaxf(mm, ml[0]);
    const float e0 = __builtin_amdgcn_exp2f((mm - M) * C), e1 = __builtin_amdgcn_exp2f((ml[0] - M) * C);
    const float inv = 1.f / (ll * e0 + ml[1] * e1);
    const float f0 = e0 * inv, f1 = e1 * inv;
    u16* dst = ows + ((size_t)(br * NH + h) * MROWS + (b0 + it) * SQ + 32 * qb + r32) * DH + 64 * kh + 8 * hh;
#pragma unroll
    for (int dd = 0; dd < 2; ++dd) {
      const f32x16& a = kh ? o[2 + dd] : o[dd];
      unsigned pk[4][2];
#pragma unroll
      for (int g = 0; g < 4; ++g) {
        const f32x4 p = *(const f32x4*)(ex + pw * 8192 + (dd * 4 + g) * 1024 + lane * 16);
        pk[g][0] = cvtpk(a[4 * g] * f0 + p[0] * f1, a[4 * g + 1] * f0 + p[1] * f1);
        pk[g][1] = cvtpk(a[4 * g + 2] * f0 + p[2] * f1, a[4 * g + 3] * f0 + p[3] * f1);
      }
#pragma unroll
      for (int gp = 0; gp < 2; ++gp) {
        auto sx = __builtin_amdgcn_permlane32_swap(pk[2 * gp][0], pk[2 * gp + 1][0], false, false);
        auto sy = __builtin_amdgcn_permlane32_swap(pk[2 * gp][1], pk[2 * gp + 1][1], false, false);
        *(u32x4*)(dst + 32 * dd + 16 * gp) = (u32x4){sx[0], sy[0], sx[1], sy[1]};
      }
    }
  };

  bar();
  f32x16 sn = {};
  {
    bf16x8 kf[8], kg[8]; readk(kf, 0); readk(kg, 1);
    f32x16 s = {};
#pragma unroll
    for (int ds = 0; ds < 8; ++ds) s = MFMA(kf[ds], qa[ds], s);
#pragma unroll
    for (int ds = 0; ds < 8; ++ds) sn = MFMA(kg[ds], qa[ds], sn);
    float alpha; softmax(s, alpha);
  }
#pragma unroll
  for (int T = 0; T < 16; ++T) {
    bar();
    bf16x8 kf[8], vf[8];
    readv(vf, T);
    if (T == 6) {
#pragma unroll
      for (int f = 0; f < 8; ++f) qa[f] = *(const bf16x8*)(smem + WQB + (qb + 3) * 8192 + f * 1024 + lane * 16);
    }
    if (T < 14) readk(kf, T + 2);
    f32x16 s = sn;
    const float mo = m, lo = l;
    if (T == 7) { m = -INFINITY; l = 0.f; }
    if (T < 14) {
      sn = (f32x16){};
#pragma unroll
      for (int i = 0; i < 8; ++i) {
        o[i & 3] = MFMA(vf[i], pf[i >> 2], o[i & 3]);
        if (i >= 2) sn = MFMA(kf[i - 2], qa[i - 2], sn);
      }
      sn = MFMA(kf[6], qa[6], sn); sn = MFMA(kf[7], qa[7], sn);
    } else pv(vf);
    float alpha = 1.f;
    bool need = false;
    if (T < 15) need = softmax(s, alpha);
    if (T < 14) {
      __builtin_amdgcn_sched_group_barrier(0x100, 24, 0);
      if (T == 6) __builtin_amdgcn_sched_group_barrier(0x100, 8, 0);
      __builtin_amdgcn_sched_group_barrier(0x002, 12, 0);
#pragma unroll
      for (int i = 0; i < 3; ++i) {
        __builtin_amdgcn_sched_group_barrier(0x008, 1, 0);
        __builtin_amdgcn_sched_group_barrier(0x002, 4, 0);
      }
#pragma unroll
      for (int i = 3; i < 16; ++i) {
        __builtin_amdgcn_sched_group_barrier(0x008, 1, 0);
        __builtin_amdgcn_sched_group_barrier(0x002, 3, 0);
        __builtin_amdgcn_sched_group_barrier(0x400, 2, 0);
      }
    }
    __builtin_amdgcn_sched_barrier(0);
    if (T == 7) {
      finish(0, mo, lo);
#pragma unroll
      for (int d = 0; d < 4; ++d) o[d] = (f32x16){};
    } else if (need) {
#pragma unroll
      for (int d = 0; d < 4; ++d)
#pragma unroll
        for (int r = 0; r < 16; ++r) o[d][r] *= alpha;
    }
  }
  finish(1, m, l);
}

constexpr int SPLITK = 4, KR = KO / SPLITK;
constexpr int GST = 12288 + 16384, GNS = 5;
__global__ __launch_bounds__(512, 2) void oproj_kernel(const u16* __restrict__ ows, const u16* __restrict__ woA,
                                                       const u16* __restrict__ woB, float* __restrict__ part) {
  __shared__ __attribute__((aligned(16))) char smem[GNS * GST];
  const int tid = threadIdx.x, lane = tid & 63;
  const int wid = __builtin_amdgcn_readfirstlane(tid >> 6);
  const int bid = blockIdx.x;
  const int xcd = bid & 7, rr = bid >> 3;
  const int br = xcd >> 2, sk = xcd & 3, mt = rr & 7, nt = rr >> 3;
  const int wn = wid & 3, wk = wid >> 2;
  const unsigned lbase = (unsigned)(uintptr_t)(lds_ptr)smem;
  const int r32 = lane & 31, hh = lane >> 5;
  const char* gsrc; unsigned v0, v1, v2, v3, ldst;
  if (wid < 4) {
    const unsigned lrow = lane >> 4;
    gsrc = (const char*)((br ? woB : woA) + ((size_t)sk * KR + 16 * wid) * DM + nt * 128);
    v0 = lrow * (DM * 2) + (((lane & 15) ^ (lrow << 2)) << 4);
    v1 = (v0 ^ 16) + 4 * (DM * 2) - 1024; v2 = (v0 ^ 32) + 8 * (DM * 2) - 2048; v3 = (v0 ^ 48) + 12 * (DM * 2) - 3072;
    ldst = lbase + 12288 + wid * 4096;
  } else {
    const unsigned lrow = lane >> 3, p0 = 3 * (wid - 4);
    gsrc = (const char*)(ows + ((size_t)(br * NH + sk * (KR / DH)) * MROWS + mt * SQ + 8 * p0) * DH);
    auto vo = [&](unsigned p) { return (8 * p + lrow) * (DH * 2) + (((lane & 7) ^ ((4 * (p0 + p) + (lrow >> 1)) & 7)) << 4) - 1024 * p; };
    v0 = vo(0); v1 = vo(1); v2 = vo(2); v3 = 0;
    ldst = lbase + p0 * 1024;
  }
  auto fill = [&](int c) {
    const unsigned dst = ldst + (c % GNS) * GST;
    if (wid < 4) glds16x4(gsrc + (size_t)c * (64 * DM * 2), v0, v1, v2, v3, dst);
    else {
      const char* s0 = gsrc + (size_t)(c >> 1) * (MROWS * DH * 2) + 128 * (c & 1);
      unsigned keep;
      asm volatile("s_mov_b32 %0, m0\n\ts_mov_b32 m0, %5\n\ts_nop 0\n\t"
                   "global_load_lds_dwordx4 %1, %4\n\t"
                   "global_load_lds_dwordx4 %2, %4 offset:1024\n\t"
                   "global_load_lds_dwordx4 %3, %4 offset:2048\n\t"
                   "s_mov_b32 m0, %0"
                   : "=&s"(keep) : "v"(v0), "v"(v1), "v"(v2), "s"(s0), "s"(dst) : "memory");
    }
  };
  auto waitst = [&](int n) {
    if (wid < 4) { if (n == 3) wait_vm<12>(); else if (n == 2) wait_vm<8>(); else if (n == 1) wait_vm<4>(); else wait_vm<0>(); }
    else { if (n == 3) wait_vm<9>(); else if (n == 2) wait_vm<6>(); else if (n == 1) wait_vm<3>(); else wait_vm<0>(); }
  };
  const unsigned blk = (lane >> 4) & 1, qq = (lane & 15) >> 2, pp = lane & 3;
  const unsigned cch = 4 * wn + 2 * blk + (pp >> 1);
  const unsigned b0a = lbase + 12288 + off_b(32 * wk + 8 * hh + qq, cch) + 8 * (pp & 1);
  const unsigned b1a = lbase + 12288 + off_b(32 * wk + 8 * hh + 4 + qq, cch) + 8 * (pp & 1);
  const unsigned aa = lbase + r32 * 128 + 16 * ((4 * wk + hh) ^ ((r32 >> 1) & 7));
  bf16x8 wf[2][2], af[2][6];
  auto rd = [&](int c) {
    const unsigned so = (c % GNS) * GST;
#pragma unroll
    for (int ks = 0; ks < 2; ++ks) {
      const s16x4 lo = vtr(b0a + so + 4096 * ks), hi = vtr(b1a + so + 4096 * ks);
      wf[c & 1][ks] = PK8(lo, hi);
#pragma unroll
      for (int mb = 0; mb < 3; ++mb) af[c & 1][3 * ks + mb] = ldsr128((aa ^ (32 * ks)) + so + 4096 * mb);
    }
  };
#pragma unroll
  for (int c = 0; c < GNS; ++c) fill(c);
  f32x16 acc[3] = {};
  if (wid < 4) wait_vm<16>(); else wait_vm<12>();
  bar();
  rd(0);
#pragma unroll
  for (int c = 0; c < 16; ++c) {
    if (c < 15) {
      waitst(c + 4 < 16 ? 3 : 14 - c);
      bar();
      if (c + GNS < 16) fill(c + GNS);
      rd(c + 1);
    }
#pragma unroll
    for (int ks = 0; ks < 2; ++ks)
#pragma unroll
      for (int mb = 0; mb < 3; ++mb) acc[mb] = MFMA(wf[c & 1][ks], af[c & 1][3 * ks + mb], acc[mb]);
    if (c < 15) {
#pragma unroll
      for (int i = 0; i < 5; ++i) {
        __builtin_amdgcn_sched_group_barrier(0x008, 1, 0);
        __builtin_amdgcn_sched_group_barrier(0x100, 2, 0);
      }
      __builtin_amdgcn_sched_group_barrier(0x008, 1, 0);
    }
    __builtin_amdgcn_sched_barrier(0);
  }
  bar();
  char* ex = smem + wn * 12288 + lane * 16;
  if (wk) {
#pragma unroll
    for (int mb = 0; mb < 3; ++mb)
#pragma unroll
      for (int g = 0; g < 4; ++g)
        *(f32x4*)(ex + (mb * 4 + g) * 1024) = (f32x4){acc[mb][4 * g], acc[mb][4 * g + 1], acc[mb][4 * g + 2], acc[mb][4 * g + 3]};
  }
  bar();
  if (!wk) {
    float* dst = part + ((((size_t)sk * 2 + br) * 32 + mt * 4 + nt) * 4 + wn) * 3072 + lane * 4;
#pragma unroll
    for (int mb = 0; mb < 3; ++mb)
#pragma unroll
      for (int g = 0; g < 4; ++g) {
        const f32x4 p = *(const f32x4*)(ex + (mb * 4 + g) * 1024);
        *(f32x4*)(dst + (mb * 4 + g) * 256) =
            (f32x4){acc[mb][4 * g] + p[0], acc[mb][4 * g + 1] + p[1], acc[mb][4 * g + 2] + p[2], acc[mb][4 * g + 3] + p[3]};
      }
  }
}

__global__ __launch_bounds__(256) void reduce_kernel(const float* __restrict__ part, u16* __restrict__ out) {
  const int i = blockIdx.x * 256 + threadIdx.x;
  const int r32 = i & 31, q = (i >> 5) % 12, tw = (i >> 5) / 12;
  const int wn = tw & 3, tile = tw >> 2;
  const int nt = tile & 3, mt = (tile >> 2) & 7, br = tile >> 5;
  const float* src = part + ((size_t)tw * 12 + q) * 256 + r32 * 4;
  f32x4 s0 = {}, s1 = {};
#pragma unroll
  for (int sk = 0; sk < SPLITK; ++sk) {
    s0 += *(const f32x4*)(src + (size_t)sk * (2 * MROWS * DM));
    s1 += *(const f32x4*)(src + (size_t)sk * (2 * MROWS * DM) + 128);
  }
  const int row = mt * SQ + 32 * (q >> 2) + r32, col = br * DM + nt * 128 + wn * 32 + 8 * (q & 3);
  *(u32x4*)(out + (size_t)row * (2 * DM) + col) = (u32x4){cvtpk(s0[0], s0[1]), cvtpk(s0[2], s0[3]), cvtpk(s1[0], s1[1]), cvtpk(s1[2], s1[3])};
}

extern "C" void kernel_launch(void* const* d_in, const int* in_sizes, int n_in,
                              void* d_out, int out_size, void* d_ws, size_t ws_size,
                              hipStream_t stream) {
  const u16* xA = (const u16*)d_in[0]; const u16* kA = (const u16*)d_in[1]; const u16* vA = (const u16*)d_in[2];
  const u16* wqA = (const u16*)d_in[3]; const u16* woA = (const u16*)d_in[4];
  const u16* xB = (const u16*)d_in[5]; const u16* kB = (const u16*)d_in[6]; const u16* vB = (const u16*)d_in[7];
  const u16* wqB = (const u16*)d_in[8]; const u16* woB = (const u16*)d_in[9];
  u16* ows = (u16*)d_ws;
  float* part = (float*)((char*)d_ws + (size_t)2 * MROWS * KO * 2);
  attn_kernel<<<256, 512, 0, stream>>>(xA, kA, vA, wqA, xB, kB, vB, wqB, ows);
  oproj_kernel<<<256, 512, 0, stream>>>(ows, woA, woB, part);
  reduce_kernel<<<2 * MROWS * DM / 8 / 256, 256, 0, stream>>>(part, (u16*)d_out);
}
```

```cpp
#include <hip/hip_runtime.h>
#include <hip/hip_bf16.h>
#include <stdint.h>

typedef unsigned short u16;
using bf16x8 = __attribute__((ext_vector_type(8))) short;
using s16x4  = __attribute__((ext_vector_type(4))) short;
using f32x16 = __attribute__((ext_vector_type(16))) float;
using f32x4  = __attribute__((ext_vector_type(4))) float;
using u32x4  = __attribute__((ext_vector_type(4))) unsigned;
using u32x2  = __attribute__((ext_vector_type(2))) unsigned;
typedef short v4i16_t __attribute__((ext_vector_type(4)));
typedef __attribute__((address_space(3))) char* lds_ptr;

constexpr int NB = 8, SQ = 96, DM = 512, NH = 32, SK = 512, DH = 128;
constexpr int MROWS = NB * SQ;
constexpr int KO = NH * DH;

__device__ __forceinline__ unsigned swz_b(unsigned row) { return ((row & 3) << 2) | ((row >> 2) & 3); }
__device__ __forceinline__ unsigned off_b(unsigned row, unsigned ch) { return 256u * row + 16u * (ch ^ swz_b(row)); }

__device__ __forceinline__ void glds16(const void* g, unsigned lds_dst) {
  unsigned keep;
  asm volatile("s_mov_b32 %0, m0\n\ts_mov_b32 m0, %2\n\ts_nop 0\n\tglobal_load_lds_dwordx4 %1, off\n\ts_mov_b32 m0, %0"
               : "=&s"(keep) : "v"(g), "s"(lds_dst) : "memory");
}
__device__ __forceinline__ void glds16_nt(const void* g, unsigned lds_dst) {
  unsigned keep;
  asm volatile("s_mov_b32 %0, m0\n\ts_mov_b32 m0, %2\n\ts_nop 0\n\tglobal_load_lds_dwordx4 %1, off nt\n\ts_mov_b32 m0, %0"
               : "=&s"(keep) : "v"(g), "s"(lds_dst) : "memory");
}
__device__ __forceinline__ void glds16x4_nt(const void* sbase, unsigned v0, unsigned v1, unsigned v2, unsigned v3, unsigned lds_dst) {
  unsigned keep;
  asm volatile("s_mov_b32 %0, m0\n\ts_mov_b32 m0, %6\n\ts_nop 0\n\t"
               "global_load_lds_dwordx4 %1, %5 nt\n\t"
               "global_load_lds_dwordx4 %2, %5 offset:1024 nt\n\t"
               "global_load_lds_dwordx4 %3, %5 offset:2048 nt\n\t"
               "global_load_lds_dwordx4 %4, %5 offset:3072 nt\n\t"
               "s_mov_b32 m0, %0"
               : "=&s"(keep) : "v"(v0), "v"(v1), "v"(v2), "v"(v3), "s"(sbase), "s"(lds_dst) : "memory");
}
template <int N> __device__ __forceinline__ void wait_vm() { asm volatile("s_waitcnt vmcnt(%0)" :: "i"(N) : "memory"); }
__device__ __forceinline__ void bar() { asm volatile("s_waitcnt lgkmcnt(0)\n\ts_barrier" ::: "memory"); }

__device__ __forceinline__ s16x4 vtr(unsigned a) {
  return __builtin_bit_cast(s16x4, __builtin_amdgcn_ds_read_tr16_b64_v4i16((__attribute__((address_space(3))) v4i16_t*)(uintptr_t)a));
}
__device__ __forceinline__ bf16x8 ldsr128(unsigned a) {
  return *(const __attribute__((address_space(3))) bf16x8*)(uintptr_t)a;
}
typedef __bf16 bf16x2_t __attribute__((ext_vector_type(2)));
typedef float f32x2_t __attribute__((ext_vector_type(2)));
__device__ __forceinline__ unsigned cvtpk(float lo, float hi) {
  return __builtin_bit_cast(unsigned, __builtin_convertvector((f32x2_t){lo, hi}, bf16x2_t));
}
__device__ __forceinline__ float swap32_max(float v) {
  auto rr = __builtin_amdgcn_permlane32_swap(__float_as_uint(v), __float_as_uint(v), false, false);
  return fmaxf(__uint_as_float(rr[0]), __uint_as_float(rr[1]));
}
__device__ __forceinline__ float swap32_sum(float v) {
  auto rr = __builtin_amdgcn_permlane32_swap(__float_as_uint(v), __float_as_uint(v), false, false);
  return __uint_as_float(rr[0]) + __uint_as_float(rr[1]);
}
#define MFMA(a, b, c) __builtin_amdgcn_mfma_f32_32x32x16_bf16(a, b, c, 0, 0, 0)
#define PK8(L, H) (bf16x8){L[0], L[1], L[2], L[3], H[0], H[1], H[2], H[3]}

constexpr int PFT = 12;
constexpr int SLOT = 16384, WQB = 6 * SLOT;
constexpr int NWQ = 5, XSB = WQB + NWQ * 8192;

__device__ __forceinline__ void glds16x4(const void* sbase, unsigned v0, unsigned v1, unsigned v2, unsigned v3, unsigned lds_dst) {
  unsigned keep;
  asm volatile("s_mov_b32 %0, m0\n\ts_mov_b32 m0, %6\n\ts_nop 0\n\t"
               "global_load_lds_dwordx4 %1, %5\n\t"
               "global_load_lds_dwordx4 %2, %5 offset:1024\n\t"
               "global_load_lds_dwordx4 %3, %5 offset:2048\n\t"
               "global_load_lds_dwordx4 %4, %5 offset:3072\n\t"
               "s_mov_b32 m0, %0"
               : "=&s"(keep) : "v"(v0), "v"(v1), "v"(v2), "v"(v3), "s"(sbase), "s"(lds_dst) : "memory");
}

__global__ __launch_bounds__(512, 2) void attn_kernel(
    const u16* __restrict__ xA, const u16* __restrict__ kA, const u16* __restrict__ vA, const u16* __restrict__ wqA,
    const u16* __restrict__ xB, const u16* __restrict__ kB, const u16* __restrict__ vB, const u16* __restrict__ wqB,
    u16* __restrict__ ows, const u16* __restrict__ woA, const u16* __restrict__ woB) {
  __shared__ __attribute__((aligned(16))) char smem[WQB + 65536];
  const int tid = threadIdx.x, lane = tid & 63;
  const int wid = __builtin_amdgcn_readfirstlane(tid >> 6);
  const int bid = blockIdx.x;
  const int xcd = bid & 7, rr = bid >> 3;
  const int br = xcd >> 2, h = (xcd & 3) * 8 + (rr >> 2), b0 = 2 * (rr & 3);
  const unsigned lbase = (unsigned)(uintptr_t)(lds_ptr)smem;
  const int r32 = lane & 31, hh = lane >> 5;

  if (wid >= 6) {
    const char* kg  = (const char*)((br ? kB : kA) + (size_t)(b0 * NH + h) * SK * DH);
    const char* vg  = (const char*)((br ? vB : vA) + (size_t)(b0 * NH + h) * SK * DH);
    const char* wqg = (const char*)((br ? wqB : wqA) + h * DH);
    constexpr size_t ITEM = (size_t)NH * SK * DH * 2;
    const unsigned lrow = lane >> 4;
    const unsigned fv0 = lrow * 256 + (((lane & 15) ^ (lrow << 2)) << 4);
    auto kvfill = [&](int f, int q0, int nq) {
      const int t = f >> 1;
      const char* s0 = ((f & 1) ? vg : kg) + (t >> 3) * ITEM + (t & 7) * SLOT;
      const unsigned dst = lbase + ((f & 1) * 3 + t % 3) * SLOT;
#pragma unroll
      for (int q = q0; q < q0 + nq; ++q) glds16x4_nt(s0 + q * 4096, fv0, fv0 ^ 16, fv0 ^ 32, fv0 ^ 48, dst + q * 4096);
    };
    u32x4 rb[3][16];
    auto rload = [&](int f) {
      const int t = f >> 1;
      const char* s0 = ((f & 1) ? vg : kg) + (t >> 3) * ITEM + (t & 7) * SLOT;
#pragma unroll
      for (int k = 0; k < 16; ++k)
        rb[((f - 6) >> 1) % 3][k] = __builtin_nontemporal_load((const u32x4*)(s0 + k * 1024 + (fv0 ^ (16 * (k & 3)))));
    };
    auto rstore = [&](int f) {
      char* dst = smem + ((f & 1) * 3 + (f >> 1) % 3) * SLOT + lane * 16;
#pragma unroll
      for (int k = 0; k < 16; ++k) *(u32x4*)(dst + k * 1024) = rb[((f - 6) >> 1) % 3][k];
    };
    if (wid == 6) {
      const unsigned w0 = lrow * (KO * 2) + (((lane & 15) ^ (lrow << 2)) << 4);
      const unsigned w1 = (w0 ^ 16) + 4 * (KO * 2) - 1024, w2 = (w0 ^ 32) + 8 * (KO * 2) - 2048, w3 = (w0 ^ 48) + 12 * (KO * 2) - 3072;
      auto wqfill = [&](int c) {
        const char* s0 = wqg + (size_t)c * (32 * KO * 2);
        const unsigned dst = lbase + WQB + (c % NWQ) * 8192;
        glds16x4(s0, w0, w1, w2, w3, dst);
        glds16x4(s0 + 16 * KO * 2, w0, w1, w2, w3, dst + 4096);
      };
#pragma unroll
      for (int c = 0; c < NWQ; ++c) wqfill(c);
      wait_vm<32>();
      bar();
#pragma unroll
      for (int c = 0; c < 16; ++c) {
        if (c < 11) wait_vm<24>(); else if (c == 11) wait_vm<40>(); else if (c == 12) wait_vm<32>();
        else if (c == 13) wait_vm<40>(); else if (c == 14) wait_vm<32>();
        bar();
        if (c + NWQ < 16) wqfill(c + NWQ);
        if (c == 10) rload(7);
        if (c == 12) rload(9);
        if (c == 14) rload(11);
      }
      bar(); bar();
      bar();
#pragma unroll
      for (int T = 0; T < 15; ++T) {
        bar();
        if (T > 0 && T < 11) { rstore(2 * T + 5); rload(2 * T + 11); }
        else if (T >= 12) rstore(2 * T + 3);
        if (T == 7) bar();
      }
    } else {
      kvfill(0, 0, 4); kvfill(2, 0, 4);
      bar();
      kvfill(1, 0, 4);
#pragma unroll
      for (int c = 0; c < 16; ++c) {
        bar();
        if (c == 3) { wait_vm<47>(); kvfill(4, 0, 4); }
        if (c == 5) { wait_vm<47>(); kvfill(3, 0, 4); }
        if (c == 7) { wait_vm<47>(); kvfill(5, 0, 4); }
        if (c == 9) { wait_vm<47>(); rload(6); }
        if (c == 11) { wait_vm<47>(); rload(8); }
        if (c == 13) { wait_vm<47>(); rload(10); }
      }
      bar(); bar();
      bar();
      u32x4 pft;
#pragma unroll
      for (int T = 0; T < 15; ++T) {
        bar();
        if (T == 0) { rstore(6); rload(12); }
        if (T < 9) { rstore(2 * T + 8); rload(2 * T + 14); }
        else if (T >= 10 && T < 13) rstore(2 * T + 6);
        if (T == 7) bar();
        if (T == PFT) {
          const char* pw = (const char*)(br ? woB : woA) + (size_t)((xcd & 3) * 32 + rr) * 32768;
          const char* pp = (const char*)ows + (size_t)2 * MROWS * KO * 2 + (size_t)(((xcd & 3) * 2 + br) * 32 + rr) * 49152;
          asm volatile("global_load_dwordx4 %0, %1, %2" : "=&v"(pft) : "v"(lane * 768), "s"(pp) : "memory");
#pragma unroll
          for (int k = 0; k < 8; ++k)
            asm volatile("global_load_dwordx4 %0, %1, %2\n\tglobal_load_dwordx4 %0, %1, %2 offset:1024\n\t"
                         "global_load_dwordx4 %0, %1, %2 offset:2048\n\tglobal_load_dwordx4 %0, %1, %2 offset:3072"
                         : "+v"(pft) : "v"(lane * 16), "s"(pw + k * 4096) : "memory");
        }
      }
      bar();
      bar();
      asm volatile("s_waitcnt vmcnt(0)" :: "v"(pft) : "memory");
      return;
    }
    bar();
    bar();
    return;
  }

  const int qb = wid % 3, kh = wid / 3;
  const unsigned blk = (lane >> 4) & 1, qq = (lane & 15) >> 2, pp = lane & 3;
  bf16x8 qa[8];
  {
    const char* xp = (const char*)((br ? xB : xA) + (size_t)b0 * SQ * DM) + (size_t)(32 * wid + (lane >> 3)) * (DM * 2) + 16 * (lane & 7);
    u32x4 xr[8][4];
    auto xload = [&](int j) {
#pragma unroll
      for (int i = 0; i < 4; ++i) xr[j][i] = *(const u32x4*)(xp + i * (8 * DM * 2) + j * 128);
    };
#pragma unroll
    for (int j = 0; j < 4; ++j) xload(j);
    char* xs = smem + XSB + wid * 4096;
    const unsigned xw0 = (lane >> 3) * 128 + 16 * ((lane & 7) ^ ((lane >> 4) & 3));
    const unsigned xrd = r32 * 128 + 16 * (hh ^ ((r32 >> 1) & 7));
    f32x16 qacc[4] = {};
    bf16x8 xf[2][4], wf[2][8];
    auto xstage = [&](int j) {
#pragma unroll
      for (int i = 0; i < 4; ++i) *(u32x4*)(xs + i * 1024 + (xw0 ^ (64 * (i & 1)))) = xr[j][i];
      if (j + 4 < 8) xload(j + 4);
#pragma unroll
      for (int s = 0; s < 4; ++s) xf[j & 1][s] = *(const bf16x8*)(xs + (xrd ^ (32 * s)));
    };
    const unsigned pp2 = ((pp & 1) << 1) | (pp >> 1);
    const unsigned a0 = lbase + WQB + off_b(8 * hh + qq, 2 * blk + (pp2 >> 1)) + 8 * (pp2 & 1);
    const unsigned a1 = lbase + WQB + off_b(8 * hh + 4 + qq, 2 * blk + (pp2 >> 1)) + 8 * (pp2 & 1);
    auto wread = [&](int c) {
#pragma unroll
      for (int s = 0; s < 2; ++s)
#pragma unroll
        for (int db = 0; db < 4; ++db) {
          const s16x4 lo = vtr((a0 ^ (64 * db)) + (c % NWQ) * 8192 + 4096 * s);
          const s16x4 hi = vtr((a1 ^ (64 * db)) + (c % NWQ) * 8192 + 4096 * s);
          wf[c & 1][4 * s + db] = PK8(lo, hi);
        }
    };
    xstage(0);
    bar();
    wread(0);
#pragma unroll
    for (int c = 0; c < 16; ++c) {
      bar();
      if (c + 1 < 16) wread(c + 1);
      if ((c & 1) && c < 15) xstage((c + 1) / 2);
      __builtin_amdgcn_sched_barrier(0);
#pragma unroll
      for (int s = 0; s < 2; ++s)
#pragma unroll
        for (int db = 0; db < 4; ++db) qacc[db] = MFMA(wf[c & 1][4 * s + db], xf[(c >> 1) & 1][2 * (c & 1) + s], qacc[db]);
      __builtin_amdgcn_sched_barrier(0);
    }
    bar();
    char* ex = smem + WQB;
#pragma unroll
    for (int db = 0; db < 4; ++db)
#pragma unroll
      for (int s = 0; s < 2; ++s) {
        const f32x16& a = qacc[db];
        const u32x4 w = {cvtpk(a[8 * s + 0], a[8 * s + 1]), cvtpk(a[8 * s + 2], a[8 * s + 3]),
                         cvtpk(a[8 * s + 4], a[8 * s + 5]), cvtpk(a[8 * s + 6], a[8 * s + 7])};
        *(u32x4*)(ex + wid * 8192 + (2 * db + s) * 1024 + lane * 16) = w;
      }
    bar();
#pragma unroll
    for (int f = 0; f < 8; ++f) qa[f] = *(const bf16x8*)(ex + qb * 8192 + f * 1024 + lane * 16);
  }

  constexpr float C = 0.08838834764831845f * 1.4426950408889634f;
  constexpr float TAU = 8.0f / C;
  const unsigned krow = 32 * kh + r32;
  const unsigned kb0 = lbase + krow * 256 + 16 * (hh ^ swz_b(krow));
  const unsigned vrow = 32 * kh + 4 * hh + qq;
  const unsigned vb0 = lbase + 3 * SLOT + vrow * 256 + 16 * ((2 * blk + (pp >> 1)) ^ swz_b(vrow)) + 8 * (pp & 1);
  const unsigned vb1 = lbase + 3 * SLOT + (vrow + 8) * 256 + 16 * ((2 * blk + (pp >> 1)) ^ swz_b(vrow + 8)) + 8 * (pp & 1);

  float m = -INFINITY, l = 0.f;
  f32x16 o[4] = {};
  bf16x8 pf[2];

  auto readk = [&](bf16x8 (&kf)[8], int t) {
#pragma unroll
    for (int ds = 0; ds < 8; ++ds) kf[ds] = ldsr128((kb0 ^ (32 * ds)) + (t % 3) * SLOT);
  };
  auto readv = [&](bf16x8 (&vf)[8], int t) {
#pragma unroll
    for (int ks = 0; ks < 2; ++ks)
#pragma unroll
      for (int d = 0; d < 4; ++d) {
        const s16x4 lo = vtr((vb0 ^ (64 * d)) + (t % 3) * SLOT + 4096 * ks);
        const s16x4 hi = vtr((vb1 ^ (64 * d)) + (t % 3) * SLOT + 4096 * ks);
        vf[4 * ks + d] = PK8(lo, hi);
      }
  };
  auto pv = [&](const bf16x8 (&vf)[8]) {
#pragma unroll
    for (int ks = 0; ks < 2; ++ks)
#pragma unroll
      for (int d = 0; d < 4; ++d) o[d] = MFMA(vf[4 * ks + d], pf[ks], o[d]);
  };
  auto softmax = [&](f32x16& s, float& alpha) -> bool {
#define MX3(a, b, c) __builtin_elementwise_maximum(__builtin_elementwise_maximum(a, b), c)
    float mx = MX3(MX3(MX3(s[0], s[1], s[2]), MX3(s[3], s[4], s[5]), MX3(s[6], s[7], s[8])),
                   MX3(MX3(s[9], s[10], s[11]), MX3(s[12], s[13], s[14]), s[15]), m);
    {
      auto rr = __builtin_amdgcn_permlane32_swap(__float_as_uint(mx), __float_as_uint(mx), false, false);
      mx = __builtin_elementwise_maximum(__uint_as_float(rr[0]), __uint_as_float(rr[1]));
    }
    const bool need = __builtin_amdgcn_ballot_w64(!(mx <= m + TAU)) != 0;
    const float mn = need ? mx : m;
    alpha = __builtin_amdgcn_exp2f((m - mn) * C);
    m = mn;
    const float mc = -mn * C;
    f32x2_t ps[4];
#pragma unroll
    for (int r = 0; r < 8; ++r) {
      const f32x2_t t = __builtin_elementwise_fma((f32x2_t){s[2 * r], s[2 * r + 1]}, (f32x2_t){C, C}, (f32x2_t){mc, mc});
      s[2 * r] = __builtin_amdgcn_exp2f(t[0]); s[2 * r + 1] = __builtin_amdgcn_exp2f(t[1]);
      if (r < 4) ps[r] = (f32x2_t){s[2 * r], s[2 * r + 1]}; else ps[r - 4] += (f32x2_t){s[2 * r], s[2 * r + 1]};
    }
    const f32x2_t pt = (ps[0] + ps[1]) + (ps[2] + ps[3]);
    l = l * alpha + (pt[0] + pt[1]);
    asm volatile("" : "+v"(l));
#pragma unroll
    for (int ks = 0; ks < 2; ++ks) {
      u32x4 w = {cvtpk(s[8 * ks + 0], s[8 * ks + 1]), cvtpk(s[8 * ks + 2], s[8 * ks + 3]),
                 cvtpk(s[8 * ks + 4], s[8 * ks + 5]), cvtpk(s[8 * ks + 6], s[8 * ks + 7])};
      pf[ks] = __builtin_bit_cast(bf16x8, w);
    }
    return need;
  };
  auto finish = [&](const int it, const float mm, float ll) {
    char* ex = smem + WQB;
    ll = swap32_sum(ll);
#pragma unroll
    for (int dd = 0; dd < 2; ++dd)
#pragma unroll
      for (int g = 0; g < 4; ++g) {
        const f32x16& a = kh ? o[dd] : o[2 + dd];
        *(f32x4*)(ex + wid * 8192 + (dd * 4 + g) * 1024 + lane * 16) = (f32x4){a[4 * g], a[4 * g + 1], a[4 * g + 2], a[4 * g + 3]};
      }
    *(f32x2_t*)(ex + 49152 + wid * 512 + lane * 8) = (f32x2_t){mm, ll};
    bar();
    const int pw = wid < 3 ? wid + 3 : wid - 3;
    const f32x2_t ml = *(const f32x2_t*)(ex + 49152 + pw * 512 + lane * 8);
    const float M = fmaxf(mm, ml[0]);
    const float e0 = __builtin_amdgcn_exp2f((mm - M) * C), e1 = __builtin_amdgcn_exp2f((ml[0] - M) * C);
    const float inv = 1.f / (ll * e0 + ml[1] * e1);
    const float f0 = e0 * inv, f1 = e1 * inv;
    u16* dst = ows + ((size_t)(br * NH + h) * MROWS + (b0 + it) * SQ + 32 * qb + r32) * DH + 64 * kh + 8 * hh;
#pragma unroll
    for (int dd = 0; dd < 2; ++dd) {
      const f32x16& a = kh ? o[2 + dd] : o[dd];
      unsigned pk[4][2];
#pragma unroll
      for (int g = 0; g < 4; ++g) {
        const f32x4 p = *(const f32x4*)(ex + pw * 8192 + (dd * 4 + g) * 1024 + lane * 16);
        pk[g][0] = cvtpk(a[4 * g] * f0 + p[0] * f1, a[4 * g + 1] * f0 + p[1] * f1);
        pk[g][1] = cvtpk(a[4 * g + 2] * f0 + p[2] * f1, a[4 * g + 3] * f0 + p[3] * f1);
      }
#pragma unroll
      for (int gp = 0; gp < 2; ++gp) {
        auto sx = __builtin_amdgcn_permlane32_swap(pk[2 * gp][0], pk[2 * gp + 1][0], false, false);
        auto sy = __builtin_amdgcn_permlane32_swap(pk[2 * gp][1], pk[2 * gp + 1][1], false, false);
        *(u32x4*)(dst + 32 * dd + 16 * gp) = (u32x4){sx[0], sy[0], sx[1], sy[1]};
      }
    }
  };

  bar();
  f32x16 sn = {};
  {
    bf16x8 kf[8], kg[8]; readk(kf, 0); readk(kg, 1);
    f32x16 s = {};
#pragma unroll
    for (int ds = 0; ds < 8; ++ds) s = MFMA(kf[ds], qa[ds], s);
#pragma unroll
    for (int ds = 0; ds < 8; ++ds) sn = MFMA(kg[ds], qa[ds], sn);
    float alpha; softmax(s, alpha);
  }
#pragma unroll
  for (int T = 0; T < 16; ++T) {
    bar();
    bf16x8 kf[8], vf[8];
    readv(vf, T);
    if (T == 6) {
#pragma unroll
      for (int f = 0; f < 8; ++f) qa[f] = *(const bf16x8*)(smem + WQB + (qb + 3) * 8192 + f * 1024 + lane * 16);
    }
    if (T < 14) readk(kf, T + 2);
    f32x16 s = sn;
    const float mo = m, lo = l;
    if (T == 7) { m = -INFINITY; l = 0.f; }
    if (T < 14) {
      sn = (f32x16){};
#pragma unroll
      for (int i = 0; i < 8; ++i) {
        o[i & 3] = MFMA(vf[i], pf[i >> 2], o[i & 3]);
        if (i >= 2) sn = MFMA(kf[i - 2], qa[i - 2], sn);
      }
      sn = MFMA(kf[6], qa[6], sn); sn = MFMA(kf[7], qa[7], sn);
    } else pv(vf);
    float alpha = 1.f;
    bool need = false;
    if (T < 15) need = softmax(s, alpha);
    if (T < 14) {
      __builtin_amdgcn_sched_group_barrier(0x100, 24, 0);
      if (T == 6) __builtin_amdgcn_sched_group_barrier(0x100, 8, 0);
      __builtin_amdgcn_sched_group_barrier(0x002, 12, 0);
#pragma unroll
      for (int i = 0; i < 3; ++i) {
        __builtin_amdgcn_sched_group_barrier(0x008, 1, 0);
        __builtin_amdgcn_sched_group_barrier(0x002, 4, 0);
      }
#pragma unroll
      for (int i = 3; i < 16; ++i) {
        __builtin_amdgcn_sched_group_barrier(0x008, 1, 0);
        __builtin_amdgcn_sched_group_barrier(0x002, 3, 0);
        __builtin_amdgcn_sched_group_barrier(0x400, 2, 0);
      }
    }
    __builtin_amdgcn_sched_barrier(0);
    if (T == 7) {
      finish(0, mo, lo);
#pragma unroll
      for (int d = 0; d < 4; ++d) o[d] = (f32x16){};
    } else if (need) {
#pragma unroll
      for (int d = 0; d < 4; ++d)
#pragma unroll
        for (int r = 0; r < 16; ++r) o[d][r] *= alpha;
    }
  }
  finish(1, m, l);
}

constexpr int SPLITK = 4, KR = KO / SPLITK;
constexpr int GST = 12288 + 16384, GNS = 5;
__global__ __launch_bounds__(512, 2) void oproj_kernel(const u16* __restrict__ ows, const u16* __restrict__ woA,
                                                       const u16* __restrict__ woB, float* __restrict__ part) {
  __shared__ __attribute__((aligned(16))) char smem[GNS * GST];
  const int tid = threadIdx.x, lane = tid & 63;
  const int wid = __builtin_amdgcn_readfirstlane(tid >> 6);
  const int bid = blockIdx.x;
  const int xcd = bid & 7, rr = bid >> 3;
  const int br = xcd >> 2, sk = xcd & 3, mt = rr & 7, nt = rr >> 3;
  const int wn = wid & 3, wk = wid >> 2;
  const unsigned lbase = (unsigned)(uintptr_t)(lds_ptr)smem;
  const int r32 = lane & 31, hh = lane >> 5;
  const char* gsrc; unsigned v0, v1, v2, v3, ldst;
  if (wid < 4) {
    const unsigned lrow = lane >> 4;
    gsrc = (const char*)((br ? woB : woA) + ((size_t)sk * KR + 16 * wid) * DM + nt * 128);
    v0 = lrow * (DM * 2) + (((lane & 15) ^ (lrow << 2)) << 4);
    v1 = (v0 ^ 16) + 4 * (DM * 2) - 1024; v2 = (v0 ^ 32) + 8 * (DM * 2) - 2048; v3 = (v0 ^ 48) + 12 * (DM * 2) - 3072;
    ldst = lbase + 12288 + wid * 4096;
  } else {
    const unsigned lrow = lane >> 3, p0 = 3 * (wid - 4);
    gsrc = (const char*)(ows + ((size_t)(br * NH + sk * (KR / DH)) * MROWS + mt * SQ + 8 * p0) * DH);
    auto vo = [&](unsigned p) { return (8 * p + lrow) * (DH * 2) + (((lane & 7) ^ ((4 * (p0 + p) + (lrow >> 1)) & 7)) << 4) - 1024 * p; };
    v0 = vo(0); v1 = vo(1); v2 = vo(2); v3 = 0;
    ldst = lbase + p0 * 1024;
  }
  auto fill = [&](int c) {
    const unsigned dst = ldst + (c % GNS) * GST;
    if (wid < 4) glds16x4(gsrc + (size_t)c * (64 * DM * 2), v0, v1, v2, v3, dst);
    else {
      const char* s0 = gsrc + (size_t)(c >> 1) * (MROWS * DH * 2) + 128 * (c & 1);
      unsigned keep;
      asm volatile("s_mov_b32 %0, m0\n\ts_mov_b32 m0, %5\n\ts_nop 0\n\t"
                   "global_load_lds_dwordx4 %1, %4\n\t"
                   "global_load_lds_dwordx4 %2, %4 offset:1024\n\t"
                   "global_load_lds_dwordx4 %3, %4 offset:2048\n\t"
                   "s_mov_b32 m0, %0"
                   : "=&s"(keep) : "v"(v0), "v"(v1), "v"(v2), "s"(s0), "s"(dst) : "memory");
    }
  };
  auto waitst = [&](int n) {
    if (wid < 4) { if (n == 3) wait_vm<12>(); else if (n == 2) wait_vm<8>(); else if (n == 1) wait_vm<4>(); else wait_vm<0>(); }
    else { if (n == 3) wait_vm<9>(); else if (n == 2) wait_vm<6>(); else if (n == 1) wait_vm<3>(); else wait_vm<0>(); }
  };
  const unsigned blk = (lane >> 4) & 1, qq = (lane & 15) >> 2, pp = lane & 3;
  const unsigned cch = 4 * wn + 2 * blk + (pp >> 1);
  const unsigned b0a = lbase + 12288 + off_b(32 * wk + 8 * hh + qq, cch) + 8 * (pp & 1);
  const unsigned b1a = lbase + 12288 + off_b(32 * wk + 8 * hh + 4 + qq, cch) + 8 * (pp & 1);
  const unsigned aa = lbase + r32 * 128 + 16 * ((4 * wk + hh) ^ ((r32 >> 1) & 7));
  bf16x8 wf[2][2], af[2][6];
  auto rd = [&](int c) {
    const unsigned so = (c % GNS) * GST;
#pragma unroll
    for (int ks = 0; ks < 2; ++ks) {
      const s16x4 lo = vtr(b0a + so + 4096 * ks), hi = vtr(b1a + so + 4096 * ks);
      wf[c & 1][ks] = PK8(lo, hi);
#pragma unroll
      for (int mb = 0; mb < 3; ++mb) af[c & 1][3 * ks + mb] = ldsr128((aa ^ (32 * ks)) + so + 4096 * mb);
    }
  };
#pragma unroll
  for (int c = 0; c < GNS; ++c) fill(c);
  f32x16 acc[3] = {};
  if (wid < 4) wait_vm<16>(); else wait_vm<12>();
  bar();
  rd(0);
#pragma unroll
  for (int c = 0; c < 16; ++c) {
    if (c < 15) {
      waitst(c + 4 < 16 ? 3 : 14 - c);
      bar();
      if (c + GNS < 16) fill(c + GNS);
      rd(c + 1);
    }
#pragma unroll
    for (int ks = 0; ks < 2; ++ks)
#pragma unroll
      for (int mb = 0; mb < 3; ++mb) acc[mb] = MFMA(wf[c & 1][ks], af[c & 1][3 * ks + mb], acc[mb]);
    if (c < 15) {
#pragma unroll
      for (int i = 0; i < 5; ++i) {
        __builtin_amdgcn_sched_group_barrier(0x008, 1, 0);
        __builtin_amdgcn_sched_group_barrier(0x100, 2, 0);
      }
      __builtin_amdgcn_sched_group_barrier(0x008, 1, 0);
    }
    __builtin_amdgcn_sched_barrier(0);
  }
  bar();
  char* ex = smem + wn * 12288 + lane * 16;
  if (wk) {
#pragma unroll
    for (int mb = 0; mb < 3; ++mb)
#pragma unroll
      for (int g = 0; g < 4; ++g)
        *(f32x4*)(ex + (mb * 4 + g) * 1024) = (f32x4){acc[mb][4 * g], acc[mb][4 * g + 1], acc[mb][4 * g + 2], acc[mb][4 * g + 3]};
  }
  bar();
  if (!wk) {
    float* dst = part + ((((size_t)sk * 2 + br) * 32 + mt * 4 + nt) * 4 + wn) * 3072 + lane * 4;
#pragma unroll
    for (int mb = 0; mb < 3; ++mb)
#pragma unroll
      for (int g = 0; g < 4; ++g) {
        const f32x4 p = *(const f32x4*)(ex + (mb * 4 + g) * 1024);
        *(f32x4*)(dst + (mb * 4 + g) * 256) =
            (f32x4){acc[mb][4 * g] + p[0], acc[mb][4 * g + 1] + p[1], acc[mb][4 * g + 2] + p[2], acc[mb][4 * g + 3] + p[3]};
      }
  }
}

__global__ __launch_bounds__(256) void reduce_kernel(const float* __restrict__ part, u16* __restrict__ out) {
  const int i = blockIdx.x * 256 + threadIdx.x;
  const int r32 = i & 31, q = (i >> 5) % 12, tw = (i >> 5) / 12;
  const int wn = tw & 3, tile = tw >> 2;
  const int nt = tile & 3, mt = (tile >> 2) & 7, br = tile >> 5;
  const float* src = part + ((size_t)tw * 12 + q) * 256 + r32 * 4;
  f32x4 s0 = {}, s1 = {};
#pragma unroll
  for (int sk = 0; sk < SPLITK; ++sk) {
    s0 += *(const f32x4*)(src + (size_t)sk * (2 * MROWS * DM));
    s1 += *(const f32x4*)(src + (size_t)sk * (2 * MROWS * DM) + 128);
  }
  const int row = mt * SQ + 32 * (q >> 2) + r32, col = br * DM + nt * 128 + wn * 32 + 8 * (q & 3);
  *(u32x4*)(out + (size_t)row * (2 * DM) + col) = (u32x4){cvtpk(s0[0], s0[1]), cvtpk(s0[2], s0[3]), cvtpk(s1[0], s1[1]), cvtpk(s1[2], s1[3])};
}

extern "C" void kernel_launch(void* const* d_in, const int* in_sizes, int n_in,
                              void* d_out, int out_size, void* d_ws, size_t ws_size,
                              hipStream_t stream) {
  const u16* xA = (const u16*)d_in[0]; const u16* kA = (const u16*)d_in[1]; const u16* vA = (const u16*)d_in[2];
  const u16* wqA = (const u16*)d_in[3]; const u16* woA = (const u16*)d_in[4];
  const u16* xB = (const u16*)d_in[5]; const u16* kB = (const u16*)d_in[6]; const u16* vB = (const u16*)d_in[7];
  const u16* wqB = (const u16*)d_in[8]; const u16* woB = (const u16*)d_in[9];
  u16* ows = (u16*)d_ws;
  float* part = (float*)((char*)d_ws + (size_t)2 * MROWS * KO * 2);
  attn_kernel<<<256, 512, 0, stream>>>(xA, kA, vA, wqA, xB, kB, vB, wqB, ows, woA, woB);
  oproj_kernel<<<256, 512, 0, stream>>>(ows, woA, woB, part);
  reduce_kernel<<<2 * MROWS * DM / 8 / 256, 256, 0, stream>>>(part, (u16*)d_out);
}
```

```cpp
#include <hip/hip_runtime.h>
#include <hip/hip_bf16.h>
#include <stdint.h>

typedef unsigned short u16;
using bf16x8 = __attribute__((ext_vector_type(8))) short;
using s16x4  = __attribute__((ext_vector_type(4))) short;
using f32x16 = __attribute__((ext_vector_type(16))) float;
using f32x4  = __attribute__((ext_vector_type(4))) float;
using u32x4  = __attribute__((ext_vector_type(4))) unsigned;
using u32x2  = __attribute__((ext_vector_type(2))) unsigned;
typedef short v4i16_t __attribute__((ext_vector_type(4)));
typedef __attribute__((address_space(3))) char* lds_ptr;

constexpr int NB = 8, SQ = 96, DM = 512, NH = 32, SK = 512, DH = 128;
constexpr int MROWS = NB * SQ;
constexpr int KO = NH * DH;

__device__ __forceinline__ unsigned swz_b(unsigned row) { return ((row & 3) << 2) | ((row >> 2) & 3); }
__device__ __forceinline__ unsigned off_b(unsigned row, unsigned ch) { return 256u * row + 16u * (ch ^ swz_b(row)); }

__device__ __forceinline__ void glds16(const void* g, unsigned lds_dst) {
  unsigned keep;
  asm volatile("s_mov_b32 %0, m0\n\ts_mov_b32 m0, %2\n\ts_nop 0\n\tglobal_load_lds_dwordx4 %1, off\n\ts_mov_b32 m0, %0"
               : "=&s"(keep) : "v"(g), "s"(lds_dst) : "memory");
}
__device__ __forceinline__ void glds16_nt(const void* g, unsigned lds_dst) {
  unsigned keep;
  asm volatile("s_mov_b32 %0, m0\n\ts_mov_b32 m0, %2\n\ts_nop 0\n\tglobal_load_lds_dwordx4 %1, off nt\n\ts_mov_b32 m0, %0"
               : "=&s"(keep) : "v"(g), "s"(lds_dst) : "memory");
}
__device__ __forceinline__ void glds16x4_nt(const void* sbase, unsigned v0, unsigned v1, unsigned v2, unsigned v3, unsigned lds_dst) {
  unsigned keep;
  asm volatile("s_mov_b32 %0, m0\n\ts_mov_b32 m0, %6\n\ts_nop 0\n\t"
               "global_load_lds_dwordx4 %1, %5 nt\n\t"
               "global_load_lds_dwordx4 %2, %5 offset:1024 nt\n\t"
               "global_load_lds_dwordx4 %3, %5 offset:2048 nt\n\t"
               "global_load_lds_dwordx4 %4, %5 offset:3072 nt\n\t"
               "s_mov_b32 m0, %0"
               : "=&s"(keep) : "v"(v0), "v"(v1), "v"(v2), "v"(v3), "s"(sbase), "s"(lds_dst) : "memory");
}
template <int N> __device__ __forceinline__ void wait_vm() { asm volatile("s_waitcnt vmcnt(%0)" :: "i"(N) : "memory"); }
__device__ __forceinline__ void bar() { asm volatile("s_waitcnt lgkmcnt(0)\n\ts_barrier" ::: "memory"); }

__device__ __forceinline__ s16x4 vtr(unsigned a) {
  return __builtin_bit_cast(s16x4, __builtin_amdgcn_ds_read_tr16_b64_v4i16((__attribute__((address_space(3))) v4i16_t*)(uintptr_t)a));
}
__device__ __forceinline__ bf16x8 ldsr128(unsigned a) {
  return *(const __attribute__((address_space(3))) bf16x8*)(uintptr_t)a;
}
typedef __bf16 bf16x2_t __attribute__((ext_vector_type(2)));
typedef float f32x2_t __attribute__((ext_vector_type(2)));
__device__ __forceinline__ unsigned cvtpk(float lo, float hi) {
  return __builtin_bit_cast(unsigned, __builtin_convertvector((f32x2_t){lo, hi}, bf16x2_t));
}
__device__ __forceinline__ float swap32_max(float v) {
  auto rr = __builtin_amdgcn_permlane32_swap(__float_as_uint(v), __float_as_uint(v), false, false);
  return fmaxf(__uint_as_float(rr[0]), __uint_as_float(rr[1]));
}
__device__ __forceinline__ float swap32_sum(float v) {
  auto rr = __builtin_amdgcn_permlane32_swap(__float_as_uint(v), __float_as_uint(v), false, false);
  return __uint_as_float(rr[0]) + __uint_as_float(rr[1]);
}
#define MFMA(a, b, c) __builtin_amdgcn_mfma_f32_32x32x16_bf16(a, b, c, 0, 0, 0)
#define PK8(L, H) (bf16x8){L[0], L[1], L[2], L[3], H[0], H[1], H[2], H[3]}

constexpr int PFT = 12;
constexpr int SLOT = 16384, WQB = 6 * SLOT;
constexpr int NWQ = 5, XSB = WQB + NWQ * 8192;

__device__ __forceinline__ void glds16x4(const void* sbase, unsigned v0, unsigned v1, unsigned v2, unsigned v3, unsigned lds_dst) {
  unsigned keep;
  asm volatile("s_mov_b32 %0, m0\n\ts_mov_b32 m0, %6\n\ts_nop 0\n\t"
               "global_load_lds_dwordx4 %1, %5\n\t"
               "global_load_lds_dwordx4 %2, %5 offset:1024\n\t"
               "global_load_lds_dwordx4 %3, %5 offset:2048\n\t"
               "global_load_lds_dwordx4 %4, %5 offset:3072\n\t"
               "s_mov_b32 m0, %0"
               : "=&s"(keep) : "v"(v0), "v"(v1), "v"(v2), "v"(v3), "s"(sbase), "s"(lds_dst) : "memory");
}

__global__ __launch_bounds__(512, 2) void attn_kernel(
    const u16* __restrict__ xA, const u16* __restrict__ kA, const u16* __restrict__ vA, const u16* __restrict__ wqA,
    const u16* __restrict__ xB, const u16* __restrict__ kB, const u16* __restrict__ vB, const u16* __restrict__ wqB,
    u16* __restrict__ ows, const u16* __restrict__ woA, const u16* __restrict__ woB) {
  __shared__ __attribute__((aligned(16))) char smem[WQB + 65536];
  const int tid = threadIdx.x, lane = tid & 63;
  const int wid = __builtin_amdgcn_readfirstlane(tid >> 6);
  const int bid = blockIdx.x;
  const int xcd = bid & 7, rr = bid >> 3;
  const int br = xcd >> 2, h = (xcd & 3) * 8 + (rr >> 2), b0 = 2 * (rr & 3);
  const unsigned lbase = (unsigned)(uintptr_t)(lds_ptr)smem;
  const int r32 = lane & 31, hh = lane >> 5;

  if (wid >= 6) {
    const char* kg  = (const char*)((br ? kB : kA) + (size_t)(b0 * NH + h) * SK * DH);
    const char* vg  = (const char*)((br ? vB : vA) + (size_t)(b0 * NH + h) * SK * DH);
    const char* wqg = (const char*)((br ? wqB : wqA) + h * DH);
    constexpr size_t ITEM = (size_t)NH * SK * DH * 2;
    const unsigned lrow = lane >> 4;
    const unsigned fv0 = lrow * 256 + (((lane & 15) ^ (lrow << 2)) << 4);
    auto kvfill = [&](int f, int q0, int nq) {
      const int t = f >> 1;
      const char* s0 = ((f & 1) ? vg : kg) + (t >> 3) * ITEM + (t & 7) * SLOT;
      const unsigned dst = lbase + ((f & 1) * 3 + t % 3) * SLOT;
#pragma unroll
      for (int q = q0; q < q0 + nq; ++q) glds16x4_nt(s0 + q * 4096, fv0, fv0 ^ 16, fv0 ^ 32, fv0 ^ 48, dst + q * 4096);
    };
    u32x4 rb[3][16];
    auto rload = [&](int f) {
      const int t = f >> 1;
      const char* s0 = ((f & 1) ? vg : kg) + (t >> 3) * ITEM + (t & 7) * SLOT;
#pragma unroll
      for (int k = 0; k < 16; ++k)
        rb[((f - 6) >> 1) % 3][k] = __builtin_nontemporal_load((const u32x4*)(s0 + k * 1024 + (fv0 ^ (16 * (k & 3)))));
    };
    auto rstore = [&](int f) {
      char* dst = smem + ((f & 1) * 3 + (f >> 1) % 3) * SLOT + lane * 16;
#pragma unroll
      for (int k = 0; k < 16; ++k) *(u32x4*)(dst + k * 1024) = rb[((f - 6) >> 1) % 3][k];
    };
    if (wid == 6) {
      const unsigned w0 = lrow * (KO * 2) + (((lane & 15) ^ (lrow << 2)) << 4);
      const unsigned w1 = (w0 ^ 16) + 4 * (KO * 2) - 1024, w2 = (w0 ^ 32) + 8 * (KO * 2) - 2048, w3 = (w0 ^ 48) + 12 * (KO * 2) - 3072;
      auto wqfill = [&](int c) {
        const char* s0 = wqg + (size_t)c * (32 * KO * 2);
        const unsigned dst = lbase + WQB + (c % NWQ) * 8192;
        glds16x4(s0, w0, w1, w2, w3, dst);
        glds16x4(s0 + 16 * KO * 2, w0, w1, w2, w3, dst + 4096);
      };
#pragma unroll
      for (int c = 0; c < NWQ; ++c) wqfill(c);
      wait_vm<32>();
      bar();
#pragma unroll
      for (int c = 0; c < 16; ++c) {
        if (c < 11) wait_vm<24>(); else if (c == 11) wait_vm<40>(); else if (c == 12) wait_vm<32>();
        else if (c == 13) wait_vm<40>(); else if (c == 14) wait_vm<32>();
        bar();
        if (c + NWQ < 16) wqfill(c + NWQ);
        if (c == 10) rload(7);
        if (c == 12) rload(9);
        if (c == 14) rload(11);
      }
      bar(); bar();
      bar();
#pragma unroll
      for (int T = 0; T < 15; ++T) {
        bar();
        if (T > 0 && T < 11) { rstore(2 * T + 5); rload(2 * T + 11); }
        else if (T >= 12) rstore(2 * T + 3);
        if (T == 7) bar();
      }
    } else {
      kvfill(0, 0, 4); kvfill(2, 0, 4);
      bar();
      kvfill(1, 0, 4);
#pragma unroll
      for (int c = 0; c < 16; ++c) {
        bar();
        if (c == 3) { wait_vm<47>(); kvfill(4, 0, 4); }
        if (c == 5) { wait_vm<47>(); kvfill(3, 0, 4); }
        if (c == 7) { wait_vm<47>(); kvfill(5, 0, 4); }
        if (c == 9) { wait_vm<47>(); rload(6); }
        if (c == 11) { wait_vm<47>(); rload(8); }
        if (c == 13) { wait_vm<47>(); rload(10); }
      }
      bar(); bar();
      bar();
      u32x4 pft;
#pragma unroll
      for (int T = 0; T < 15; ++T) {
        bar();
        if (T == 0) { rstore(6); rload(12); }
        if (T < 9) { rstore(2 * T + 8); rload(2 * T + 14); }
        else if (T >= 10 && T < 13) rstore(2 * T + 6);
        if (T == 7) bar();
        if (T == PFT || T == PFT + 1) {
          const char* pw = (const char*)(br ? woB : woA) + (size_t)(128 * rr + 64 * (T - PFT)) * 1024 + 512 * (xcd & 1);
          const unsigned pv = (lane >> 5) * 1024 + 16 * (lane & 31);
          if (T == PFT) asm volatile("global_load_dwordx4 %0, %1, %2" : "=&v"(pft) : "v"(pv), "s"(pw) : "memory");
#pragma unroll
          for (int k = 0; k < 16; ++k)
            asm volatile("global_load_dwordx4 %0, %1, %2\n\tglobal_load_dwordx4 %0, %1, %2 offset:2048"
                         : "+v"(pft) : "v"(pv), "s"(pw + k * 4096) : "memory");
        }
      }
      bar();
      bar();
      asm volatile("s_waitcnt vmcnt(0)" :: "v"(pft) : "memory");
      return;
    }
    bar();
    bar();
    return;
  }

  const int qb = wid % 3, kh = wid / 3;
  const unsigned blk = (lane >> 4) & 1, qq = (lane & 15) >> 2, pp = lane & 3;
  bf16x8 qa[8];
  {
    const char* xp = (const char*)((br ? xB : xA) + (size_t)b0 * SQ * DM) + (size_t)(32 * wid + (lane >> 3)) * (DM * 2) + 16 * (lane & 7);
    u32x4 xr[8][4];
    auto xload = [&](int j) {
#pragma unroll
      for (int i = 0; i < 4; ++i) xr[j][i] = *(const u32x4*)(xp + i * (8 * DM * 2) + j * 128);
    };
#pragma unroll
    for (int j = 0; j < 4; ++j) xload(j);
    char* xs = smem + XSB + wid * 4096;
    const unsigned xw0 = (lane >> 3) * 128 + 16 * ((lane & 7) ^ ((lane >> 4) & 3));
    const unsigned xrd = r32 * 128 + 16 * (hh ^ ((r32 >> 1) & 7));
    f32x16 qacc[4] = {};
    bf16x8 xf[2][4], wf[2][8];
    auto xstage = [&](int j) {
#pragma unroll
      for (int i = 0; i < 4; ++i) *(u32x4*)(xs + i * 1024 + (xw0 ^ (64 * (i & 1)))) = xr[j][i];
      if (j + 4 < 8) xload(j + 4);
#pragma unroll
      for (int s = 0; s < 4; ++s) xf[j & 1][s] = *(const bf16x8*)(xs + (xrd ^ (32 * s)));
    };
    const unsigned pp2 = ((pp & 1) << 1) | (pp >> 1);
    const unsigned a0 = lbase + WQB + off_b(8 * hh + qq, 2 * blk + (pp2 >> 1)) + 8 * (pp2 & 1);
    const unsigned a1 = lbase + WQB + off_b(8 * hh + 4 + qq, 2 * blk + (pp2 >> 1)) + 8 * (pp2 & 1);
    auto wread = [&](int c) {
#pragma unroll
      for (int s = 0; s < 2; ++s)
#pragma unroll
        for (int db = 0; db < 4; ++db) {
          const s16x4 lo = vtr((a0 ^ (64 * db)) + (c % NWQ) * 8192 + 4096 * s);
          const s16x4 hi = vtr((a1 ^ (64 * db)) + (c % NWQ) * 8192 + 4096 * s);
          wf[c & 1][4 * s + db] = PK8(lo, hi);
        }
    };
    xstage(0);
    bar();
    wread(0);
#pragma unroll
    for (int c = 0; c < 16; ++c) {
      bar();
      if (c + 1 < 16) wread(c + 1);
      if ((c & 1) && c < 15) xstage((c + 1) / 2);
      __builtin_amdgcn_sched_barrier(0);
#pragma unroll
      for (int s = 0; s < 2; ++s)
#pragma unroll
        for (int db = 0; db < 4; ++db) qacc[db] = MFMA(wf[c & 1][4 * s + db], xf[(c >> 1) & 1][2 * (c & 1) + s], qacc[db]);
      __builtin_amdgcn_sched_barrier(0);
    }
    bar();
    char* ex = smem + WQB;
#pragma unroll
    for (int db = 0; db < 4; ++db)
#pragma unroll
      for (int s = 0; s < 2; ++s) {
        const f32x16& a = qacc[db];
        const u32x4 w = {cvtpk(a[8 * s + 0], a[8 * s + 1]), cvtpk(a[8 * s + 2], a[8 * s + 3]),
                         cvtpk(a[8 * s + 4], a[8 * s + 5]), cvtpk(a[8 * s + 6], a[8 * s + 7])};
        *(u32x4*)(ex + wid * 8192 + (2 * db + s) * 1024 + lane * 16) = w;
      }
    bar();
#pragma unroll
    for (int f = 0; f < 8; ++f) qa[f] = *(const bf16x8*)(ex + qb * 8192 + f * 1024 + lane * 16);
  }

  constexpr float C = 0.08838834764831845f * 1.4426950408889634f;
  constexpr float TAU = 8.0f / C;
  const unsigned krow = 32 * kh + r32;
  const unsigned kb0 = lbase + krow * 256 + 16 * (hh ^ swz_b(krow));
  const unsigned vrow = 32 * kh + 4 * hh + qq;
  const unsigned vb0 = lbase + 3 * SLOT + vrow * 256 + 16 * ((2 * blk + (pp >> 1)) ^ swz_b(vrow)) + 8 * (pp & 1);
  const unsigned vb1 = lbase + 3 * SLOT + (vrow + 8) * 256 + 16 * ((2 * blk + (pp >> 1)) ^ swz_b(vrow + 8)) + 8 * (pp & 1);

  float m = -INFINITY, l = 0.f;
  f32x16 o[4] = {};
  bf16x8 pf[2];

  auto readk = [&](bf16x8 (&kf)[8], int t) {
#pragma unroll
    for (int ds = 0; ds < 8; ++ds) kf[ds] = ldsr128((kb0 ^ (32 * ds)) + (t % 3) * SLOT);
  };
  auto readv = [&](bf16x8 (&vf)[8], int t) {
#pragma unroll
    for (int ks = 0; ks < 2; ++ks)
#pragma unroll
      for (int d = 0; d < 4; ++d) {
        const s16x4 lo = vtr((vb0 ^ (64 * d)) + (t % 3) * SLOT + 4096 * ks);
        const s16x4 hi = vtr((vb1 ^ (64 * d)) + (t % 3) * SLOT + 4096 * ks);
        vf[4 * ks + d] = PK8(lo, hi);
      }
  };
  auto pv = [&](const bf16x8 (&vf)[8]) {
#pragma unroll
    for (int ks = 0; ks < 2; ++ks)
#pragma unroll
      for (int d = 0; d < 4; ++d) o[d] = MFMA(vf[4 * ks + d], pf[ks], o[d]);
  };
  auto softmax = [&](f32x16& s, float& alpha) -> bool {
#define MX3(a, b, c) __builtin_elementwise_maximum(__builtin_elementwise_maximum(a, b), c)
    float mx = MX3(MX3(MX3(s[0], s[1], s[2]), MX3(s[3], s[4], s[5]), MX3(s[6], s[7], s[8])),
                   MX3(MX3(s[9], s[10], s[11]), MX3(s[12], s[13], s[14]), s[15]), m);
    {
      auto rr = __builtin_amdgcn_permlane32_swap(__float_as_uint(mx), __float_as_uint(mx), false, false);
      mx = __builtin_elementwise_maximum(__uint_as_float(rr[0]), __uint_as_float(rr[1]));
    }
    const bool need = __builtin_amdgcn_ballot_w64(!(mx <= m + TAU)) != 0;
    const float mn = need ? mx : m;
    alpha = __builtin_amdgcn_exp2f((m - mn) * C);
    m = mn;
    const float mc = -mn * C;
    f32x2_t ps[4];
#pragma unroll
    for (int r = 0; r < 8; ++r) {
      const f32x2_t t = __builtin_elementwise_fma((f32x2_t){s[2 * r], s[2 * r + 1]}, (f32x2_t){C, C}, (f32x2_t){mc, mc});
      s[2 * r] = __builtin_amdgcn_exp2f(t[0]); s[2 * r + 1] = __builtin_amdgcn_exp2f(t[1]);
      if (r < 4) ps[r] = (f32x2_t){s[2 * r], s[2 * r + 1]}; else ps[r - 4] += (f32x2_t){s[2 * r], s[2 * r + 1]};
    }
    const f32x2_t pt = (ps[0] + ps[1]) + (ps[2] + ps[3]);
    l = l * alpha + (pt[0] + pt[1]);
    asm volatile("" : "+v"(l));
#pragma unroll
    for (int ks = 0; ks < 2; ++ks) {
      u32x4 w = {cvtpk(s[8 * ks + 0], s[8 * ks + 1]), cvtpk(s[8 * ks + 2], s[8 * ks + 3]),
                 cvtpk(s[8 * ks + 4], s[8 * ks + 5]), cvtpk(s[8 * ks + 6], s[8 * ks + 7])};
      pf[ks] = __builtin_bit_cast(bf16x8, w);
    }
    return need;
  };
  auto finish = [&](const int it, const float mm, float ll) {
    char* ex = smem + WQB;
    ll = swap32_sum(ll);
#pragma unroll
    for (int dd = 0; dd < 2; ++dd)
#pragma unroll
      for (int g = 0; g < 4; ++g) {
        const f32x16& a = kh ? o[dd] : o[2 + dd];
        *(f32x4*)(ex + wid * 8192 + (dd * 4 + g) * 1024 + lane * 16) = (f32x4){a[4 * g], a[4 * g + 1], a[4 * g + 2], a[4 * g + 3]};
      }
    *(f32x2_t*)(ex + 49152 + wid * 512 + lane * 8) = (f32x2_t){mm, ll};
    bar();
    const int pw = wid < 3 ? wid + 3 : wid - 3;
    const f32x2_t ml = *(const f32x2_t*)(ex + 49152 + pw * 512 + lane * 8);
    const float M = fmaxf(mm, ml[0]);
    const float e0 = __builtin_amdgcn_exp2f((mm - M) * C), e1 = __builtin_amdgcn_exp2f((ml[0] - M) * C);
    const float inv = 1.f / (ll * e0 + ml[1] * e1);
    const float f0 = e0 * inv, f1 = e1 * inv;
    const int rbb = 2 * qb + (r32 >> 4), half = rbb >= 3;
    u16* dst = ows + ((((size_t)(br * 16 + 2 * (b0 + it) + half) * NH + h) * 4 + 2 * kh) * 3 + (rbb - 3 * half)) * 512 +
               (hh * 16 + (r32 & 15)) * 8;
#pragma unroll
    for (int dd = 0; dd < 2; ++dd) {
      const f32x16& a = kh ? o[2 + dd] : o[dd];
      unsigned pk[4][2];
#pragma unroll
      for (int g = 0; g < 4; ++g) {
        const f32x4 p = *(const f32x4*)(ex + pw * 8192 + (dd * 4 + g) * 1024 + lane * 16);
        pk[g][0] = cvtpk(a[4 * g] * f0 + p[0] * f1, a[4 * g + 1] * f0 + p[1] * f1);
        pk[g][1] = cvtpk(a[4 * g + 2] * f0 + p[2] * f1, a[4 * g + 3] * f0 + p[3] * f1);
      }
#pragma unroll
      for (int gp = 0; gp < 2; ++gp) {
        auto sx = __builtin_amdgcn_permlane32_swap(pk[2 * gp][0], pk[2 * gp + 1][0], false, false);
        auto sy = __builtin_amdgcn_permlane32_swap(pk[2 * gp][1], pk[2 * gp + 1][1], false, false);
        *(u32x4*)(dst + 1536 * dd + 256 * gp) = (u32x4){sx[0], sy[0], sx[1], sy[1]};
      }
    }
  };

  bar();
  f32x16 sn = {};
  {
    bf16x8 kf[8], kg[8]; readk(kf, 0); readk(kg, 1);
    f32x16 s = {};
#pragma unroll
    for (int ds = 0; ds < 8; ++ds) s = MFMA(kf[ds], qa[ds], s);
#pragma unroll
    for (int ds = 0; ds < 8; ++ds) sn = MFMA(kg[ds], qa[ds], sn);
    float alpha; softmax(s, alpha);
  }
#pragma unroll
  for (int T = 0; T < 16; ++T) {
    bar();
    bf16x8 kf[8], vf[8];
    readv(vf, T);
    if (T == 6) {
#pragma unroll
      for (int f = 0; f < 8; ++f) qa[f] = *(const bf16x8*)(smem + WQB + (qb + 3) * 8192 + f * 1024 + lane * 16);
    }
    if (T < 14) readk(kf, T + 2);
    f32x16 s = sn;
    const float mo = m, lo = l;
    if (T == 7) { m = -INFINITY; l = 0.f; }
    if (T < 14) {
      sn = (f32x16){};
#pragma unroll
      for (int i = 0; i < 8; ++i) {
        o[i & 3] = MFMA(vf[i], pf[i >> 2], o[i & 3]);
        if (i >= 2) sn = MFMA(kf[i - 2], qa[i - 2], sn);
      }
      sn = MFMA(kf[6], qa[6], sn); sn = MFMA(kf[7], qa[7], sn);
    } else pv(vf);
    float alpha = 1.f;
    bool need = false;
    if (T < 15) need = softmax(s, alpha);
    if (T < 14) {
      __builtin_amdgcn_sched_group_barrier(0x100, 24, 0);
      if (T == 6) __builtin_amdgcn_sched_group_barrier(0x100, 8, 0);
      __builtin_amdgcn_sched_group_barrier(0x002, 12, 0);
#pragma unroll
      for (int i = 0; i < 3; ++i) {
        __builtin_amdgcn_sched_group_barrier(0x008, 1, 0);
        __builtin_amdgcn_sched_group_barrier(0x002, 4, 0);
      }
#pragma unroll
      for (int i = 3; i < 16; ++i) {
        __builtin_amdgcn_sched_group_barrier(0x008, 1, 0);
        __builtin_amdgcn_sched_group_barrier(0x002, 3, 0);
        __builtin_amdgcn_sched_group_barrier(0x400, 2, 0);
      }
    }
    __builtin_amdgcn_sched_barrier(0);
    if (T == 7) {
      finish(0, mo, lo);
#pragma unroll
      for (int d = 0; d < 4; ++d) o[d] = (f32x16){};
    } else if (need) {
#pragma unroll
      for (int d = 0; d < 4; ++d)
#pragma unroll
        for (int r = 0; r < 16; ++r) o[d][r] *= alpha;
    }
  }
  finish(1, m, l);
}

constexpr int ONS = 5, OPA = 8;
#define MFMA16(a, b, c) __builtin_amdgcn_mfma_f32_16x16x32_bf16(a, b, c, 0, 0, 0)
constexpr int oproj_after(int j, int s) {
  int n = 0; bool seen = false;
  for (int c = 0; c < OPA; ++c) {
    if (c < ONS) { if (seen) n += 4; if (c == j) seen = true; }
    if (seen) n += 3;
  }
  for (int t = 0; t < s; ++t) {
    if (t + ONS < 16) { if (seen) n += 4; if (t + ONS == j) seen = true; }
    if (t + OPA < 16 && seen) n += 3;
  }
  return n;
}
__global__ __launch_bounds__(512, 2) void oproj_kernel(const u16* __restrict__ ows, const u16* __restrict__ woA,
                                                       const u16* __restrict__ woB, u16* __restrict__ out) {
  __shared__ __attribute__((aligned(256))) char smem[8 * ONS * 4096];
  const int tid = threadIdx.x, lane = tid & 63;
  const int wid = __builtin_amdgcn_readfirstlane(tid >> 6);
  const int bid = blockIdx.x;
  const int xcd = bid & 7, rr = bid >> 3;
  const int br = xcd >> 2, mt = 8 * ((xcd >> 1) & 1) + (rr & 7), nt = 4 * (xcd & 1) + (rr >> 3);
  const unsigned lb = (unsigned)(uintptr_t)(lds_ptr)smem + wid * (ONS * 4096);
  const char* ag = (const char*)ows + (size_t)((br * 16 + mt) * NH + 4 * wid) * 12288;
  const char* bg = (const char*)((br ? woB : woA) + (size_t)(512 * wid) * DM + nt * 64);
  const unsigned bv0 = (lane >> 3) * 1024 + 16 * ((lane & 7) ^ ((lane >> 3) & 2));
  const unsigned bv1 = (bv0 ^ 64) + 7168, bv2 = bv0 + 2 * 7168, bv3 = (bv0 ^ 64) + 3 * 7168;
  auto fillb = [&](int s) { glds16x4(bg + (size_t)s * 32768, bv0, bv1, bv2, bv3, lb + (s % ONS) * 4096); };
  bf16x8 af[OPA][3], wf[2][4];
  auto loada = [&](int s) {
    asm volatile("global_load_dwordx4 %0, %3, %4\n\tglobal_load_dwordx4 %1, %3, %4 offset:1024\n\tglobal_load_dwordx4 %2, %3, %4 offset:2048"
                 : "=&v"(af[s % OPA][0]), "=&v"(af[s % OPA][1]), "=&v"(af[s % OPA][2]) : "v"(lane * 16), "s"(ag + s * 3072) : "memory");
  };
  const unsigned g = lane >> 4, q = (lane >> 2) & 3;
  const unsigned t0 = lb + (8 * g + q) * 128 + 32 * ((q >> 1) | ((g & 1) << 1)) + 8 * (lane & 3);
  auto rdb = [&](int s) {
#pragma unroll
    for (int nb = 0; nb < 4; ++nb) {
      const s16x4 lo = vtr((t0 ^ (32 * nb)) + (s % ONS) * 4096), hi = vtr((t0 ^ (32 * nb)) + (s % ONS) * 4096 + 512);
      wf[s & 1][nb] = PK8(lo, hi);
    }
  };
#pragma unroll
  for (int c = 0; c < OPA; ++c) { if (c < ONS) fillb(c); loada(c); }
  f32x4 acc[3][4] = {};
  wait_vm<oproj_after(0, 0)>();
  rdb(0);
#pragma unroll
  for (int s = 0; s < 16; ++s) {
    if (s < 15) {
      asm volatile("s_waitcnt vmcnt(%0) lgkmcnt(0)" :: "i"(oproj_after(s + 1, s)) : "memory");
      if (s + ONS < 16) fillb(s + ONS);
      rdb(s + 1);
    }
    asm volatile("" : "+v"(af[s % OPA][0]), "+v"(af[s % OPA][1]), "+v"(af[s % OPA][2]));
#pragma unroll
    for (int i = 0; i < 3; ++i)
#pragma unroll
      for (int nb = 0; nb < 4; ++nb) acc[i][nb] = MFMA16(wf[s & 1][nb], af[s % OPA][i], acc[i][nb]);
    __builtin_amdgcn_sched_barrier(0);
    if (s + OPA < 16) loada(s + OPA);
    __builtin_amdgcn_sched_barrier(0);
  }
  bar();
#pragma unroll
  for (int i = 0; i < 3; ++i)
#pragma unroll
    for (int nb = 0; nb < 4; ++nb) *(f32x4*)(smem + wid * 12288 + (i * 4 + nb) * 1024 + lane * 16) = acc[i][nb];
  bar();
  if (tid < 384) {
    const int row = tid >> 3, c8 = tid & 7;
    const char* p = smem + ((row >> 4) * 4 + (c8 >> 1)) * 1024 + ((c8 & 1) * 32 + (row & 15)) * 16;
    f32x4 s0 = *(const f32x4*)p, s1 = *(const f32x4*)(p + 256);
#pragma unroll
    for (int w = 1; w < 8; ++w) { s0 += *(const f32x4*)(p + w * 12288); s1 += *(const f32x4*)(p + w * 12288 + 256); }
    *(u32x4*)(out + (size_t)(mt * 48 + row) * (2 * DM) + br * DM + nt * 64 + 8 * c8) =
        (u32x4){cvtpk(s0[0], s0[1]), cvtpk(s0[2], s0[3]), cvtpk(s1[0], s1[1]), cvtpk(s1[2], s1[3])};
  }
}

extern "C" void kernel_launch(void* const* d_in, const int* in_sizes, int n_in,
                              void* d_out, int out_size, void* d_ws, size_t ws_size,
                              hipStream_t stream) {
  const u16* xA = (const u16*)d_in[0]; const u16* kA = (const u16*)d_in[1]; const u16* vA = (const u16*)d_in[2];
  const u16* wqA = (const u16*)d_in[3]; const u16* woA = (const u16*)d_in[4];
  const u16* xB = (const u16*)d_in[5]; const u16* kB = (const u16*)d_in[6]; const u16* vB = (const u16*)d_in[7];
  const u16* wqB = (const u16*)d_in[8]; const u16* woB = (const u16*)d_in[9];
  u16* ows = (u16*)d_ws;
  attn_kernel<<<256, 512, 0, stream>>>(xA, kA, vA, wqA, xB, kB, vB, wqB, ows, woA, woB);
  oproj_kernel<<<256, 512, 0, stream>>>(ows, woA, woB, (u16*)d_out);
}
```

```cpp
#include <hip/hip_runtime.h>
#include <hip/hip_bf16.h>
#include <stdint.h>

typedef unsigned short u16;
using bf16x8 = __attribute__((ext_vector_type(8))) short;
using s16x4  = __attribute__((ext_vector_type(4))) short;
using f32x16 = __attribute__((ext_vector_type(16))) float;
using f32x4  = __attribute__((ext_vector_type(4))) float;
using u32x4  = __attribute__((ext_vector_type(4))) unsigned;
using u32x2  = __attribute__((ext_vector_type(2))) unsigned;
typedef short v4i16_t __attribute__((ext_vector_type(4)));
typedef __attribute__((address_space(3))) char* lds_ptr;

constexpr int NB = 8, SQ = 96, DM = 512, NH = 32, SK = 512, DH = 128;
constexpr int MROWS = NB * SQ;
constexpr int KO = NH * DH;

__device__ __forceinline__ unsigned swz_b(unsigned row) { return ((row & 3) << 2) | ((row >> 2) & 3); }
__device__ __forceinline__ unsigned off_b(unsigned row, unsigned ch) { return 256u * row + 16u * (ch ^ swz_b(row)); }

__device__ __forceinline__ void glds16(const void* g, unsigned lds_dst) {
  unsigned keep;
  asm volatile("s_mov_b32 %0, m0\n\ts_mov_b32 m0, %2\n\ts_nop 0\n\tglobal_load_lds_dwordx4 %1, off\n\ts_mov_b32 m0, %0"
               : "=&s"(keep) : "v"(g), "s"(lds_dst) : "memory");
}
__device__ __forceinline__ void glds16_nt(const void* g, unsigned lds_dst) {
  unsigned keep;
  asm volatile("s_mov_b32 %0, m0\n\ts_mov_b32 m0, %2\n\ts_nop 0\n\tglobal_load_lds_dwordx4 %1, off nt\n\ts_mov_b32 m0, %0"
               : "=&s"(keep) : "v"(g), "s"(lds_dst) : "memory");
}
__device__ __forceinline__ void glds16x4_nt(const void* sbase, unsigned v0, unsigned v1, unsigned v2, unsigned v3, unsigned lds_dst) {
  unsigned keep;
  asm volatile("s_mov_b32 %0, m0\n\ts_mov_b32 m0, %6\n\ts_nop 0\n\t"
               "global_load_lds_dwordx4 %1, %5 nt\n\t"
               "global_load_lds_dwordx4 %2, %5 offset:1024 nt\n\t"
               "global_load_lds_dwordx4 %3, %5 offset:2048 nt\n\t"
               "global_load_lds_dwordx4 %4, %5 offset:3072 nt\n\t"
               "s_mov_b32 m0, %0"
               : "=&s"(keep) : "v"(v0), "v"(v1), "v"(v2), "v"(v3), "s"(sbase), "s"(lds_dst) : "memory");
}
template <int N> __device__ __forceinline__ void wait_vm() { asm volatile("s_waitcnt vmcnt(%0)" :: "i"(N) : "memory"); }
__device__ __forceinline__ void bar() { asm volatile("s_waitcnt lgkmcnt(0)\n\ts_barrier" ::: "memory"); }

__device__ __forceinline__ s16x4 vtr(unsigned a) {
  return __builtin_bit_cast(s16x4, __builtin_amdgcn_ds_read_tr16_b64_v4i16((__attribute__((address_space(3))) v4i16_t*)(uintptr_t)a));
}
__device__ __forceinline__ bf16x8 ldsr128(unsigned a) {
  return *(const __attribute__((address_space(3))) bf16x8*)(uintptr_t)a;
}
typedef __bf16 bf16x2_t __attribute__((ext_vector_type(2)));
typedef float f32x2_t __attribute__((ext_vector_type(2)));
__device__ __forceinline__ unsigned cvtpk(float lo, float hi) {
  return __builtin_bit_cast(unsigned, __builtin_convertvector((f32x2_t){lo, hi}, bf16x2_t));
}
__device__ __forceinline__ float swap32_max(float v) {
  auto rr = __builtin_amdgcn_permlane32_swap(__float_as_uint(v), __float_as_uint(v), false, false);
  return fmaxf(__uint_as_float(rr[0]), __uint_as_float(rr[1]));
}
__device__ __forceinline__ float swap32_sum(float v) {
  auto rr = __builtin_amdgcn_permlane32_swap(__float_as_uint(v), __float_as_uint(v), false, false);
  return __uint_as_float(rr[0]) + __uint_as_float(rr[1]);
}
#define MFMA(a, b, c) __builtin_amdgcn_mfma_f32_32x32x16_bf16(a, b, c, 0, 0, 0)
#define PK8(L, H) (bf16x8){L[0], L[1], L[2], L[3], H[0], H[1], H[2], H[3]}

constexpr int PFT = 12;
constexpr int SLOT = 16384, WQB = 6 * SLOT;
constexpr int NWQ = 5, XSB = WQB + NWQ * 8192;

__device__ __forceinline__ void glds16x4(const void* sbase, unsigned v0, unsigned v1, unsigned v2, unsigned v3, unsigned lds_dst) {
  unsigned keep;
  asm volatile("s_mov_b32 %0, m0\n\ts_mov_b32 m0, %6\n\ts_nop 0\n\t"
               "global_load_lds_dwordx4 %1, %5\n\t"
               "global_load_lds_dwordx4 %2, %5 offset:1024\n\t"
               "global_load_lds_dwordx4 %3, %5 offset:2048\n\t"
               "global_load_lds_dwordx4 %4, %5 offset:3072\n\t"
               "s_mov_b32 m0, %0"
               : "=&s"(keep) : "v"(v0), "v"(v1), "v"(v2), "v"(v3), "s"(sbase), "s"(lds_dst) : "memory");
}

__global__ __launch_bounds__(512, 2) void attn_kernel(
    const u16* __restrict__ xA, const u16* __restrict__ kA, const u16* __restrict__ vA, const u16* __restrict__ wqA,
    const u16* __restrict__ xB, const u16* __restrict__ kB, const u16* __restrict__ vB, const u16* __restrict__ wqB,
    u16* __restrict__ ows, const u16* __restrict__ woA, const u16* __restrict__ woB) {
  __shared__ __attribute__((aligned(16))) char smem[WQB + 65536];
  const int tid = threadIdx.x, lane = tid & 63;
  const int wid = __builtin_amdgcn_readfirstlane(tid >> 6);
  const int bid = blockIdx.x;
  const int xcd = bid & 7, rr = bid >> 3;
  const int br = xcd >> 2, h = (xcd & 3) * 8 + (rr >> 2), b0 = 2 * (rr & 3);
  const unsigned lbase = (unsigned)(uintptr_t)(lds_ptr)smem;
  const int r32 = lane & 31, hh = lane >> 5;
  const unsigned long long pt0 = __builtin_amdgcn_s_memrealtime(), pc0 = __builtin_amdgcn_s_memtime();

  if (wid >= 6) {
    const char* kg  = (const char*)((br ? kB : kA) + (size_t)(b0 * NH + h) * SK * DH);
    const char* vg  = (const char*)((br ? vB : vA) + (size_t)(b0 * NH + h) * SK * DH);
    const char* wqg = (const char*)((br ? wqB : wqA) + h * DH);
    constexpr size_t ITEM = (size_t)NH * SK * DH * 2;
    const unsigned lrow = lane >> 4;
    const unsigned fv0 = lrow * 256 + (((lane & 15) ^ (lrow << 2)) << 4);
    auto kvfill = [&](int f, int q0, int nq) {
      const int t = f >> 1;
      const char* s0 = ((f & 1) ? vg : kg) + (t >> 3) * ITEM + (t & 7) * SLOT;
      const unsigned dst = lbase + ((f & 1) * 3 + t % 3) * SLOT;
#pragma unroll
      for (int q = q0; q < q0 + nq; ++q) glds16x4_nt(s0 + q * 4096, fv0, fv0 ^ 16, fv0 ^ 32, fv0 ^ 48, dst + q * 4096);
    };
    u32x4 rb[3][16];
    auto rload = [&](int f) {
      const int t = f >> 1;
      const char* s0 = ((f & 1) ? vg : kg) + (t >> 3) * ITEM + (t & 7) * SLOT;
#pragma unroll
      for (int k = 0; k < 16; ++k)
        rb[((f - 6) >> 1) % 3][k] = __builtin_nontemporal_load((const u32x4*)(s0 + k * 1024 + (fv0 ^ (16 * (k & 3)))));
    };
    auto rstore = [&](int f) {
      char* dst = smem + ((f & 1) * 3 + (f >> 1) % 3) * SLOT + lane * 16;
#pragma unroll
      for (int k = 0; k < 16; ++k) *(u32x4*)(dst + k * 1024) = rb[((f - 6) >> 1) % 3][k];
    };
    if (wid == 6) {
      const unsigned w0 = lrow * (KO * 2) + (((lane & 15) ^ (lrow << 2)) << 4);
      const unsigned w1 = (w0 ^ 16) + 4 * (KO * 2) - 1024, w2 = (w0 ^ 32) + 8 * (KO * 2) - 2048, w3 = (w0 ^ 48) + 12 * (KO * 2) - 3072;
      auto wqfill = [&](int c) {
        const char* s0 = wqg + (size_t)c * (32 * KO * 2);
        const unsigned dst = lbase + WQB + (c % NWQ) * 8192;
        glds16x4(s0, w0, w1, w2, w3, dst);
        glds16x4(s0 + 16 * KO * 2, w0, w1, w2, w3, dst + 4096);
      };
#pragma unroll
      for (int c = 0; c < NWQ; ++c) wqfill(c);
      wait_vm<32>();
      bar();
#pragma unroll
      for (int c = 0; c < 16; ++c) {
        if (c < 11) wait_vm<24>(); else if (c == 11) wait_vm<40>(); else if (c == 12) wait_vm<32>();
        else if (c == 13) wait_vm<40>(); else if (c == 14) wait_vm<32>();
        bar();
        if (c + NWQ < 16) wqfill(c + NWQ);
        if (c == 10) rload(7);
        if (c == 12) rload(9);
        if (c == 14) rload(11);
      }
      bar(); bar();
      bar();
#pragma unroll
      for (int T = 0; T < 15; ++T) {
        bar();
        if (T > 0 && T < 11) { rstore(2 * T + 5); rload(2 * T + 11); }
        else if (T >= 12) rstore(2 * T + 3);
        if (T == 7) bar();
      }
    } else {
      kvfill(0, 0, 4); kvfill(2, 0, 4);
      bar();
      kvfill(1, 0, 4);
#pragma unroll
      for (int c = 0; c < 16; ++c) {
        bar();
        if (c == 3) { wait_vm<47>(); kvfill(4, 0, 4); }
        if (c == 5) { wait_vm<47>(); kvfill(3, 0, 4); }
        if (c == 7) { wait_vm<47>(); kvfill(5, 0, 4); }
        if (c == 9) { wait_vm<47>(); rload(6); }
        if (c == 11) { wait_vm<47>(); rload(8); }
        if (c == 13) { wait_vm<47>(); rload(10); }
      }
      bar(); bar();
      bar();
      u32x4 pft;
#pragma unroll
      for (int T = 0; T < 15; ++T) {
        bar();
        if (T == 0) { rstore(6); rload(12); }
        if (T < 9) { rstore(2 * T + 8); rload(2 * T + 14); }
        else if (T >= 10 && T < 13) rstore(2 * T + 6);
        if (T == 7) bar();
        if (T == PFT || T == PFT + 1) {
          const char* pw = (const char*)(br ? woB : woA) + (size_t)(128 * rr + 64 * (T - PFT)) * 1024 + 512 * (xcd & 1);
          const unsigned pv = (lane >> 5) * 1024 + 16 * (lane & 31);
          if (T == PFT) asm volatile("global_load_dwordx4 %0, %1, %2" : "=&v"(pft) : "v"(pv), "s"(pw) : "memory");
#pragma unroll
          for (int k = 0; k < 16; ++k)
            asm volatile("global_load_dwordx4 %0, %1, %2\n\tglobal_load_dwordx4 %0, %1, %2 offset:2048"
                         : "+v"(pft) : "v"(pv), "s"(pw + k * 4096) : "memory");
        }
      }
      bar();
      bar();
      asm volatile("s_waitcnt vmcnt(0)" :: "v"(pft) : "memory");
      return;
    }
    bar();
    bar();
    return;
  }

  const int qb = wid % 3, kh = wid / 3;
  const unsigned blk = (lane >> 4) & 1, qq = (lane & 15) >> 2, pp = lane & 3;
  bf16x8 qa[8];
  {
    const char* xp = (const char*)((br ? xB : xA) + (size_t)b0 * SQ * DM) + (size_t)(32 * wid + (lane >> 3)) * (DM * 2) + 16 * (lane & 7);
    u32x4 xr[8][4];
    auto xload = [&](int j) {
#pragma unroll
      for (int i = 0; i < 4; ++i) xr[j][i] = *(const u32x4*)(xp + i * (8 * DM * 2) + j * 128);
    };
#pragma unroll
    for (int j = 0; j < 4; ++j) xload(j);
    char* xs = smem + XSB + wid * 4096;
    const unsigned xw0 = (lane >> 3) * 128 + 16 * ((lane & 7) ^ ((lane >> 4) & 3));
    const unsigned xrd = r32 * 128 + 16 * (hh ^ ((r32 >> 1) & 7));
    f32x16 qacc[4] = {};
    bf16x8 xf[2][4], wf[2][8];
    auto xstage = [&](int j) {
#pragma unroll
      for (int i = 0; i < 4; ++i) *(u32x4*)(xs + i * 1024 + (xw0 ^ (64 * (i & 1)))) = xr[j][i];
      if (j + 4 < 8) xload(j + 4);
#pragma unroll
      for (int s = 0; s < 4; ++s) xf[j & 1][s] = *(const bf16x8*)(xs + (xrd ^ (32 * s)));
    };
    const unsigned pp2 = ((pp & 1) << 1) | (pp >> 1);
    const unsigned a0 = lbase + WQB + off_b(8 * hh + qq, 2 * blk + (pp2 >> 1)) + 8 * (pp2 & 1);
    const unsigned a1 = lbase + WQB + off_b(8 * hh + 4 + qq, 2 * blk + (pp2 >> 1)) + 8 * (pp2 & 1);
    auto wread = [&](int c) {
#pragma unroll
      for (int s = 0; s < 2; ++s)
#pragma unroll
        for (int db = 0; db < 4; ++db) {
          const s16x4 lo = vtr((a0 ^ (64 * db)) + (c % NWQ) * 8192 + 4096 * s);
          const s16x4 hi = vtr((a1 ^ (64 * db)) + (c % NWQ) * 8192 + 4096 * s);
          wf[c & 1][4 * s + db] = PK8(lo, hi);
        }
    };
    xstage(0);
    bar();
    wread(0);
#pragma unroll
    for (int c = 0; c < 16; ++c) {
      bar();
      if (c + 1 < 16) wread(c + 1);
      if ((c & 1) && c < 15) xstage((c + 1) / 2);
      __builtin_amdgcn_sched_barrier(0);
#pragma unroll
      for (int s = 0; s < 2; ++s)
#pragma unroll
        for (int db = 0; db < 4; ++db) qacc[db] = MFMA(wf[c & 1][4 * s + db], xf[(c >> 1) & 1][2 * (c & 1) + s], qacc[db]);
      __builtin_amdgcn_sched_barrier(0);
    }
    bar();
    char* ex = smem + WQB;
#pragma unroll
    for (int db = 0; db < 4; ++db)
#pragma unroll
      for (int s = 0; s < 2; ++s) {
        const f32x16& a = qacc[db];
        const u32x4 w = {cvtpk(a[8 * s + 0], a[8 * s + 1]), cvtpk(a[8 * s + 2], a[8 * s + 3]),
                         cvtpk(a[8 * s + 4], a[8 * s + 5]), cvtpk(a[8 * s + 6], a[8 * s + 7])};
        *(u32x4*)(ex + wid * 8192 + (2 * db + s) * 1024 + lane * 16) = w;
      }
    bar();
#pragma unroll
    for (int f = 0; f < 8; ++f) qa[f] = *(const bf16x8*)(ex + qb * 8192 + f * 1024 + lane * 16);
  }

  constexpr float C = 0.08838834764831845f * 1.4426950408889634f;
  constexpr float TAU = 8.0f / C;
  const unsigned krow = 32 * kh + r32;
  const unsigned kb0 = lbase + krow * 256 + 16 * (hh ^ swz_b(krow));
  const unsigned vrow = 32 * kh + 4 * hh + qq;
  const unsigned vb0 = lbase + 3 * SLOT + vrow * 256 + 16 * ((2 * blk + (pp >> 1)) ^ swz_b(vrow)) + 8 * (pp & 1);
  const unsigned vb1 = lbase + 3 * SLOT + (vrow + 8) * 256 + 16 * ((2 * blk + (pp >> 1)) ^ swz_b(vrow + 8)) + 8 * (pp & 1);

  float m = -INFINITY, l = 0.f;
  f32x16 o[4] = {};
  bf16x8 pf[2];

  auto readk = [&](bf16x8 (&kf)[8], int t) {
#pragma unroll
    for (int ds = 0; ds < 8; ++ds) kf[ds] = ldsr128((kb0 ^ (32 * ds)) + (t % 3) * SLOT);
  };
  auto readv = [&](bf16x8 (&vf)[8], int t) {
#pragma unroll
    for (int ks = 0; ks < 2; ++ks)
#pragma unroll
      for (int d = 0; d < 4; ++d) {
        const s16x4 lo = vtr((vb0 ^ (64 * d)) + (t % 3) * SLOT + 4096 * ks);
        const s16x4 hi = vtr((vb1 ^ (64 * d)) + (t % 3) * SLOT + 4096 * ks);
        vf[4 * ks + d] = PK8(lo, hi);
      }
  };
  auto pv = [&](const bf16x8 (&vf)[8]) {
#pragma unroll
    for (int ks = 0; ks < 2; ++ks)
#pragma unroll
      for (int d = 0; d < 4; ++d) o[d] = MFMA(vf[4 * ks + d], pf[ks], o[d]);
  };
  auto softmax = [&](f32x16& s, float& alpha) -> bool {
#define MX3(a, b, c) __builtin_elementwise_maximum(__builtin_elementwise_maximum(a, b), c)
    float mx = MX3(MX3(MX3(s[0], s[1], s[2]), MX3(s[3], s[4], s[5]), MX3(s[6], s[7], s[8])),
                   MX3(MX3(s[9], s[10], s[11]), MX3(s[12], s[13], s[14]), s[15]), m);
    {
      auto rr = __builtin_amdgcn_permlane32_swap(__float_as_uint(mx), __float_as_uint(mx), false, false);
      mx = __builtin_elementwise_maximum(__uint_as_float(rr[0]), __uint_as_float(rr[1]));
    }
    const bool need = __builtin_amdgcn_ballot_w64(!(mx <= m + TAU)) != 0;
    const float mn = need ? mx : m;
    alpha = __builtin_amdgcn_exp2f((m - mn) * C);
    m = mn;
    const float mc = -mn * C;
    f32x2_t ps[4];
#pragma unroll
    for (int r = 0; r < 8; ++r) {
      const f32x2_t t = __builtin_elementwise_fma((f32x2_t){s[2 * r], s[2 * r + 1]}, (f32x2_t){C, C}, (f32x2_t){mc, mc});
      s[2 * r] = __builtin_amdgcn_exp2f(t[0]); s[2 * r + 1] = __builtin_amdgcn_exp2f(t[1]);
      if (r < 4) ps[r] = (f32x2_t){s[2 * r], s[2 * r + 1]}; else ps[r - 4] += (f32x2_t){s[2 * r], s[2 * r + 1]};
    }
    const f32x2_t pt = (ps[0] + ps[1]) + (ps[2] + ps[3]);
    l = l * alpha + (pt[0] + pt[1]);
    asm volatile("" : "+v"(l));
#pragma unroll
    for (int ks = 0; ks < 2; ++ks) {
      u32x4 w = {cvtpk(s[8 * ks + 0], s[8 * ks + 1]), cvtpk(s[8 * ks + 2], s[8 * ks + 3]),
                 cvtpk(s[8 * ks + 4], s[8 * ks + 5]), cvtpk(s[8 * ks + 6], s[8 * ks + 7])};
      pf[ks] = __builtin_bit_cast(bf16x8, w);
    }
    return need;
  };
  auto finish = [&](const int it, const float mm, float ll) {
    char* ex = smem + WQB;
    ll = swap32_sum(ll);
#pragma unroll
    for (int dd = 0; dd < 2; ++dd)
#pragma unroll
      for (int g = 0; g < 4; ++g) {
        const f32x16& a = kh ? o[dd] : o[2 + dd];
        *(f32x4*)(ex + wid * 8192 + (dd * 4 + g) * 1024 + lane * 16) = (f32x4){a[4 * g], a[4 * g + 1], a[4 * g + 2], a[4 * g + 3]};
      }
    *(f32x2_t*)(ex + 49152 + wid * 512 + lane * 8) = (f32x2_t){mm, ll};
    bar();
    const int pw = wid < 3 ? wid + 3 : wid - 3;
    const f32x2_t ml = *(const f32x2_t*)(ex + 49152 + pw * 512 + lane * 8);
    const float M = fmaxf(mm, ml[0]);
    const float e0 = __builtin_amdgcn_exp2f((mm - M) * C), e1 = __builtin_amdgcn_exp2f((ml[0] - M) * C);
    const float inv = 1.f / (ll * e0 + ml[1] * e1);
    const float f0 = e0 * inv, f1 = e1 * inv;
    const int rbb = 2 * qb + (r32 >> 4), half = rbb >= 3;
    u16* dst = ows + ((((size_t)(br * 16 + 2 * (b0 + it) + half) * NH + h) * 4 + 2 * kh) * 3 + (rbb - 3 * half)) * 512 +
               (hh * 16 + (r32 & 15)) * 8;
#pragma unroll
    for (int dd = 0; dd < 2; ++dd) {
      const f32x16& a = kh ? o[2 + dd] : o[dd];
      unsigned pk[4][2];
#pragma unroll
      for (int g = 0; g < 4; ++g) {
        const f32x4 p = *(const f32x4*)(ex + pw * 8192 + (dd * 4 + g) * 1024 + lane * 16);
        pk[g][0] = cvtpk(a[4 * g] * f0 + p[0] * f1, a[4 * g + 1] * f0 + p[1] * f1);
        pk[g][1] = cvtpk(a[4 * g + 2] * f0 + p[2] * f1, a[4 * g + 3] * f0 + p[3] * f1);
      }
#pragma unroll
      for (int gp = 0; gp < 2; ++gp) {
        auto sx = __builtin_amdgcn_permlane32_swap(pk[2 * gp][0], pk[2 * gp + 1][0], false, false);
        auto sy = __builtin_amdgcn_permlane32_swap(pk[2 * gp][1], pk[2 * gp + 1][1], false, false);
        *(u32x4*)(dst + 1536 * dd + 256 * gp) = (u32x4){sx[0], sy[0], sx[1], sy[1]};
      }
    }
  };

  bar();
  f32x16 sn = {};
  {
    bf16x8 kf[8], kg[8]; readk(kf, 0); readk(kg, 1);
    f32x16 s = {};
#pragma unroll
    for (int ds = 0; ds < 8; ++ds) s = MFMA(kf[ds], qa[ds], s);
#pragma unroll
    for (int ds = 0; ds < 8; ++ds) sn = MFMA(kg[ds], qa[ds], sn);
    float alpha; softmax(s, alpha);
  }
#pragma unroll
  for (int T = 0; T < 16; ++T) {
    bar();
    bf16x8 kf[8], vf[8];
    readv(vf, T);
    if (T == 6) {
#pragma unroll
      for (int f = 0; f < 8; ++f) qa[f] = *(const bf16x8*)(smem + WQB + (qb + 3) * 8192 + f * 1024 + lane * 16);
    }
    if (T < 14) readk(kf, T + 2);
    f32x16 s = sn;
    const float mo = m, lo = l;
    if (T == 7) { m = -INFINITY; l = 0.f; }
    if (T < 14) {
      sn = (f32x16){};
#pragma unroll
      for (int i = 0; i < 8; ++i) {
        o[i & 3] = MFMA(vf[i], pf[i >> 2], o[i & 3]);
        if (i >= 2) sn = MFMA(kf[i - 2], qa[i - 2], sn);
      }
      sn = MFMA(kf[6], qa[6], sn); sn = MFMA(kf[7], qa[7], sn);
    } else pv(vf);
    float alpha = 1.f;
    bool need = false;
    if (T < 15) need = softmax(s, alpha);
    if (T < 14) {
      __builtin_amdgcn_sched_group_barrier(0x100, 24, 0);
      if (T == 6) __builtin_amdgcn_sched_group_barrier(0x100, 8, 0);
      __builtin_amdgcn_sched_group_barrier(0x002, 12, 0);
#pragma unroll
      for (int i = 0; i < 3; ++i) {
        __builtin_amdgcn_sched_group_barrier(0x008, 1, 0);
        __builtin_amdgcn_sched_group_barrier(0x002, 4, 0);
      }
#pragma unroll
      for (int i = 3; i < 16; ++i) {
        __builtin_amdgcn_sched_group_barrier(0x008, 1, 0);
        __builtin_amdgcn_sched_group_barrier(0x002, 3, 0);
        __builtin_amdgcn_sched_group_barrier(0x400, 2, 0);
      }
    }
    __builtin_amdgcn_sched_barrier(0);
    if (T == 7) {
      finish(0, mo, lo);
#pragma unroll
      for (int d = 0; d < 4; ++d) o[d] = (f32x16){};
    } else if (need) {
#pragma unroll
      for (int d = 0; d < 4; ++d)
#pragma unroll
        for (int r = 0; r < 16; ++r) o[d][r] *= alpha;
    }
  }
  finish(1, m, l);
  if (tid == 0) {
    unsigned* pq = (unsigned*)((char*)ows + (64 << 20)) + bid * 4;
    const unsigned long long pt1 = __builtin_amdgcn_s_memrealtime(), pc1 = __builtin_amdgcn_s_memtime();
    *(u32x4*)pq = (u32x4){(unsigned)pt0, (unsigned)pt1, (unsigned)(pc1 - pc0), 0u};
  }
}

constexpr int ONS = 5, OPA = 8;
#define MFMA16(a, b, c) __builtin_amdgcn_mfma_f32_16x16x32_bf16(a, b, c, 0, 0, 0)
constexpr int oproj_after(int j, int s) {
  int n = 0; bool seen = false;
  for (int c = 0; c < OPA; ++c) {
    if (c < ONS) { if (seen) n += 4; if (c == j) seen = true; }
    if (seen) n += 3;
  }
  for (int t = 0; t < s; ++t) {
    if (t + ONS < 16) { if (seen) n += 4; if (t + ONS == j) seen = true; }
    if (t + OPA < 16 && seen) n += 3;
  }
  return n;
}
__global__ __launch_bounds__(512, 2) void oproj_kernel(const u16* __restrict__ ows, const u16* __restrict__ woA,
                                                       const u16* __restrict__ woB, u16* __restrict__ out) {
  __shared__ __attribute__((aligned(256))) char smem[8 * ONS * 4096];
  const int tid = threadIdx.x, lane = tid & 63;
  const int wid = __builtin_amdgcn_readfirstlane(tid >> 6);
  const int bid = blockIdx.x;
  const int xcd = bid & 7, rr = bid >> 3;
  const int br = xcd >> 2, mt = 8 * ((xcd >> 1) & 1) + (rr & 7), nt = 4 * (xcd & 1) + (rr >> 3);
  const unsigned lb = (unsigned)(uintptr_t)(lds_ptr)smem + wid * (ONS * 4096);
  const unsigned ot0 = (unsigned)__builtin_amdgcn_s_memrealtime(); const unsigned oc0 = (unsigned)__builtin_amdgcn_s_memtime();
  const char* ag = (const char*)ows + (size_t)((br * 16 + mt) * NH + 4 * wid) * 12288;
  const char* bg = (const char*)((br ? woB : woA) + (size_t)(512 * wid) * DM + nt * 64);
  const unsigned bv0 = (lane >> 3) * 1024 + 16 * ((lane & 7) ^ ((lane >> 3) & 2));
  const unsigned bv1 = (bv0 ^ 64) + 7168, bv2 = bv0 + 2 * 7168, bv3 = (bv0 ^ 64) + 3 * 7168;
  auto fillb = [&](int s) { glds16x4(bg + (size_t)s * 32768, bv0, bv1, bv2, bv3, lb + (s % ONS) * 4096); };
  bf16x8 af[OPA][3], wf[2][4];
  auto loada = [&](int s) {
    asm volatile("global_load_dwordx4 %0, %3, %4\n\tglobal_load_dwordx4 %1, %3, %4 offset:1024\n\tglobal_load_dwordx4 %2, %3, %4 offset:2048"
                 : "=&v"(af[s % OPA][0]), "=&v"(af[s % OPA][1]), "=&v"(af[s % OPA][2]) : "v"(lane * 16), "s"(ag + s * 3072) : "memory");
  };
  const unsigned g = lane >> 4, q = (lane >> 2) & 3;
  const unsigned t0 = lb + (8 * g + q) * 128 + 32 * ((q >> 1) | ((g & 1) << 1)) + 8 * (lane & 3);
  auto rdb = [&](int s) {
#pragma unroll
    for (int nb = 0; nb < 4; ++nb) {
      const s16x4 lo = vtr((t0 ^ (32 * nb)) + (s % ONS) * 4096), hi = vtr((t0 ^ (32 * nb)) + (s % ONS) * 4096 + 512);
      wf[s & 1][nb] = PK8(lo, hi);
    }
  };
#pragma unroll
  for (int c = 0; c < OPA; ++c) { if (c < ONS) fillb(c); loada(c); }
  f32x4 acc[3][4] = {};
  wait_vm<oproj_after(0, 0)>();
  rdb(0);
#pragma unroll
  for (int s = 0; s < 16; ++s) {
    if (s < 15) {
      asm volatile("s_waitcnt vmcnt(%0) lgkmcnt(0)" :: "i"(oproj_after(s + 1, s)) : "memory");
      if (s + ONS < 16) fillb(s + ONS);
      rdb(s + 1);
    }
    asm volatile("" : "+v"(af[s % OPA][0]), "+v"(af[s % OPA][1]), "+v"(af[s % OPA][2]));
#pragma unroll
    for (int i = 0; i < 3; ++i)
#pragma unroll
      for (int nb = 0; nb < 4; ++nb) acc[i][nb] = MFMA16(wf[s & 1][nb], af[s % OPA][i], acc[i][nb]);
    __builtin_amdgcn_sched_barrier(0);
    if (s + OPA < 16) loada(s + OPA);
    __builtin_amdgcn_sched_barrier(0);
  }
  bar();
#pragma unroll
  for (int i = 0; i < 3; ++i)
#pragma unroll
    for (int nb = 0; nb < 4; ++nb) *(f32x4*)(smem + wid * 12288 + (i * 4 + nb) * 1024 + lane * 16) = acc[i][nb];
  bar();
  if (tid < 384) {
    const int row = tid >> 3, c8 = tid & 7;
    const char* p = smem + ((row >> 4) * 4 + (c8 >> 1)) * 1024 + ((c8 & 1) * 32 + (row & 15)) * 16;
    f32x4 s0 = *(const f32x4*)p, s1 = *(const f32x4*)(p + 256);
#pragma unroll
    for (int w = 1; w < 8; ++w) { s0 += *(const f32x4*)(p + w * 12288); s1 += *(const f32x4*)(p + w * 12288 + 256); }
    *(u32x4*)(out + (size_t)(mt * 48 + row) * (2 * DM) + br * DM + nt * 64 + 8 * c8) =
        (u32x4){cvtpk(s0[0], s0[1]), cvtpk(s0[2], s0[3]), cvtpk(s1[0], s1[1]), cvtpk(s1[2], s1[3])};
  }
  if (bid == 0 && tid == 0) {
    const unsigned ot1 = (unsigned)__builtin_amdgcn_s_memrealtime(); const unsigned oc1 = (unsigned)__builtin_amdgcn_s_memtime();
    const u32x4* pq = (const u32x4*)((const char*)ows + (64 << 20));
    const u32x4 z = pq[0];
    int mn = 0, mx = 0;
    for (int i = 0; i < 256; ++i) { const u32x4 v = pq[i]; mn = min(mn, (int)(v[0] - z[0])); mx = max(mx, (int)(v[1] - z[0])); }
    int X = 2000;

    X = min(max(X, 0), 20000);
    while ((int)((unsigned)__builtin_amdgcn_s_memrealtime() - ot1) < X) __builtin_amdgcn_s_sleep(2);
  }
}

extern "C" void kernel_launch(void* const* d_in, const int* in_sizes, int n_in,
                              void* d_out, int out_size, void* d_ws, size_t ws_size,
                              hipStream_t stream) {
  const u16* xA = (const u16*)d_in[0]; const u16* kA = (const u16*)d_in[1]; const u16* vA = (const u16*)d_in[2];
  const u16* wqA = (const u16*)d_in[3]; const u16* woA = (const u16*)d_in[4];
  const u16* xB = (const u16*)d_in[5]; const u16* kB = (const u16*)d_in[6]; const u16* vB = (const u16*)d_in[7];
  const u16* wqB = (const u16*)d_in[8]; const u16* woB = (const u16*)d_in[9];
  u16* ows = (u16*)d_ws;
  attn_kernel<<<256, 512, 0, stream>>>(xA, kA, vA, wqA, xB, kB, vB, wqB, ows, woA, woB);
  oproj_kernel<<<256, 512, 0, stream>>>(ows, woA, woB, (u16*)d_out);
}
```

```cpp
#include <hip/hip_runtime.h>
#include <hip/hip_bf16.h>
#include <stdint.h>

typedef unsigned short u16;
using bf16x8 = __attribute__((ext_vector_type(8))) short;
using s16x4  = __attribute__((ext_vector_type(4))) short;
using f32x16 = __attribute__((ext_vector_type(16))) float;
using f32x4  = __attribute__((ext_vector_type(4))) float;
using u32x4  = __attribute__((ext_vector_type(4))) unsigned;
using u32x2  = __attribute__((ext_vector_type(2))) unsigned;
typedef short v4i16_t __attribute__((ext_vector_type(4)));
typedef __attribute__((address_space(3))) char* lds_ptr;

constexpr int NB = 8, SQ = 96, DM = 512, NH = 32, SK = 512, DH = 128;
constexpr int MROWS = NB * SQ;
constexpr int KO = NH * DH;

__device__ __forceinline__ unsigned swz_b(unsigned row) { return ((row & 3) << 2) | ((row >> 2) & 3); }
__device__ __forceinline__ unsigned off_b(unsigned row, unsigned ch) { return 256u * row + 16u * (ch ^ swz_b(row)); }

__device__ __forceinline__ void glds16(const void* g, unsigned lds_dst) {
  unsigned keep;
  asm volatile("s_mov_b32 %0, m0\n\ts_mov_b32 m0, %2\n\ts_nop 0\n\tglobal_load_lds_dwordx4 %1, off\n\ts_mov_b32 m0, %0"
               : "=&s"(keep) : "v"(g), "s"(lds_dst) : "memory");
}
__device__ __forceinline__ void glds16_nt(const void* g, unsigned lds_dst) {
  unsigned keep;
  asm volatile("s_mov_b32 %0, m0\n\ts_mov_b32 m0, %2\n\ts_nop 0\n\tglobal_load_lds_dwordx4 %1, off nt\n\ts_mov_b32 m0, %0"
               : "=&s"(keep) : "v"(g), "s"(lds_dst) : "memory");
}
__device__ __forceinline__ void glds16x4_nt(const void* sbase, unsigned v0, unsigned v1, unsigned v2, unsigned v3, unsigned lds_dst) {
  unsigned keep;
  asm volatile("s_mov_b32 %0, m0\n\ts_mov_b32 m0, %6\n\ts_nop 0\n\t"
               "global_load_lds_dwordx4 %1, %5 nt\n\t"
               "global_load_lds_dwordx4 %2, %5 offset:1024 nt\n\t"
               "global_load_lds_dwordx4 %3, %5 offset:2048 nt\n\t"
               "global_load_lds_dwordx4 %4, %5 offset:3072 nt\n\t"
               "s_mov_b32 m0, %0"
               : "=&s"(keep) : "v"(v0), "v"(v1), "v"(v2), "v"(v3), "s"(sbase), "s"(lds_dst) : "memory");
}
template <int N> __device__ __forceinline__ void wait_vm() { asm volatile("s_waitcnt vmcnt(%0)" :: "i"(N) : "memory"); }
__device__ __forceinline__ void bar() { asm volatile("s_waitcnt lgkmcnt(0)\n\ts_barrier" ::: "memory"); }

__device__ __forceinline__ s16x4 vtr(unsigned a) {
  return __builtin_bit_cast(s16x4, __builtin_amdgcn_ds_read_tr16_b64_v4i16((__attribute__((address_space(3))) v4i16_t*)(uintptr_t)a));
}
__device__ __forceinline__ bf16x8 ldsr128(unsigned a) {
  return *(const __attribute__((address_space(3))) bf16x8*)(uintptr_t)a;
}
typedef __bf16 bf16x2_t __attribute__((ext_vector_type(2)));
typedef float f32x2_t __attribute__((ext_vector_type(2)));
__device__ __forceinline__ unsigned cvtpk(float lo, float hi) {
  return __builtin_bit_cast(unsigned, __builtin_convertvector((f32x2_t){lo, hi}, bf16x2_t));
}
__device__ __forceinline__ float swap32_max(float v) {
  auto rr = __builtin_amdgcn_permlane32_swap(__float_as_uint(v), __float_as_uint(v), false, false);
  return fmaxf(__uint_as_float(rr[0]), __uint_as_float(rr[1]));
}
__device__ __forceinline__ float swap32_sum(float v) {
  auto rr = __builtin_amdgcn_permlane32_swap(__float_as_uint(v), __float_as_uint(v), false, false);
  return __uint_as_float(rr[0]) + __uint_as_float(rr[1]);
}
#define MFMA(a, b, c) __builtin_amdgcn_mfma_f32_32x32x16_bf16(a, b, c, 0, 0, 0)
#define PK8(L, H) (bf16x8){L[0], L[1], L[2], L[3], H[0], H[1], H[2], H[3]}

constexpr int PFT = 12;
constexpr int SLOT = 16384, WQB = 6 * SLOT;
constexpr int NWQ = 5, XSB = WQB + NWQ * 8192;

__device__ __forceinline__ void glds16x4(const void* sbase, unsigned v0, unsigned v1, unsigned v2, unsigned v3, unsigned lds_dst) {
  unsigned keep;
  asm volatile("s_mov_b32 %0, m0\n\ts_mov_b32 m0, %6\n\ts_nop 0\n\t"
               "global_load_lds_dwordx4 %1, %5\n\t"
               "global_load_lds_dwordx4 %2, %5 offset:1024\n\t"
               "global_load_lds_dwordx4 %3, %5 offset:2048\n\t"
               "global_load_lds_dwordx4 %4, %5 offset:3072\n\t"
               "s_mov_b32 m0, %0"
               : "=&s"(keep) : "v"(v0), "v"(v1), "v"(v2), "v"(v3), "s"(sbase), "s"(lds_dst) : "memory");
}


constexpr int ONS = 5, OPA = 8;
#define MFMA16(a, b, c) __builtin_amdgcn_mfma_f32_16x16x32_bf16(a, b, c, 0, 0, 0)
constexpr int oproj_wait(int s) {
  int nw = 0, na = 0; bool sw = false, sa = false;
  for (int c = 0; c < ONS; ++c) { if (sw) nw += 4; if (c == s + 1) sw = true; }
  for (int c = 0; c < OPA; ++c) { if (sw) nw += 3; if (sa) na += 3; if (c == s) sa = true; }
  for (int t = 0; t < s; ++t) {
    if (t + ONS < 16) { if (sw) nw += 4; if (sa) na += 4; if (t + ONS == s + 1) sw = true; }
    if (t + OPA < 16) { if (sw) nw += 3; if (sa) na += 3; if (t + OPA == s) sa = true; }
  }
  if (s + 1 >= 16) return na;
  return nw < na ? nw : na;
}
constexpr size_t FLAG_OFF = 16u << 20;
constexpr int NREP = 4;
constexpr unsigned TMO0 = 30000, TMO1 = 500, SLACK = 200;
typedef unsigned long long u64;
typedef volatile __attribute__((address_space(3))) unsigned* lds_word;
__device__ __forceinline__ void lds_st(unsigned a, unsigned v) { *(lds_word)(uintptr_t)a = v; }
__device__ __forceinline__ unsigned lds_ld(unsigned a) { return *(lds_word)(uintptr_t)a; }

__global__ __launch_bounds__(512, 2) void attn_kernel(
    const u16* __restrict__ xA, const u16* __restrict__ kA, const u16* __restrict__ vA, const u16* __restrict__ wqA,
    const u16* __restrict__ xB, const u16* __restrict__ kB, const u16* __restrict__ vB, const u16* __restrict__ wqB,
    u16* __restrict__ ows, const u16* __restrict__ woA, const u16* __restrict__ woB, u16* __restrict__ out) {
  __shared__ __attribute__((aligned(16))) char smem[WQB + 65536];
  const u64 tstart = __builtin_amdgcn_s_memrealtime();
  const int wid0 = __builtin_amdgcn_readfirstlane(threadIdx.x >> 6);
  const int bid0 = blockIdx.x;
  const unsigned lbase = (unsigned)(uintptr_t)(lds_ptr)smem;
  u64* const flags = (u64*)((char*)ows + FLAG_OFF);
  int item = bid0;
  unsigned patience = TMO0;
  for (;;) {
  {
  int lane;
  asm volatile("v_mbcnt_lo_u32_b32 %0, -1, 0\n\tv_mbcnt_hi_u32_b32 %0, -1, %0" : "=v"(lane));
  int wid = wid0;
  asm volatile("" : "+s"(wid));
  const int xcd = item & 7, rr = item >> 3;
  const int br = xcd >> 2, h = (xcd & 3) * 8 + (rr >> 2), b0 = 2 * (rr & 3);
  const int r32 = lane & 31, hh = lane >> 5;

  if (wid >= 6) {
    const char* kg  = (const char*)((br ? kB : kA) + (size_t)(b0 * NH + h) * SK * DH);
    const char* vg  = (const char*)((br ? vB : vA) + (size_t)(b0 * NH + h) * SK * DH);
    const char* wqg = (const char*)((br ? wqB : wqA) + h * DH);
    constexpr size_t ITEM = (size_t)NH * SK * DH * 2;
    const unsigned lrow = lane >> 4;
    const unsigned fv0 = lrow * 256 + (((lane & 15) ^ (lrow << 2)) << 4);
    auto kvfill = [&](int f, int q0, int nq) {
      const int t = f >> 1;
      const char* s0 = ((f & 1) ? vg : kg) + (t >> 3) * ITEM + (t & 7) * SLOT;
      const unsigned dst = lbase + ((f & 1) * 3 + t % 3) * SLOT;
#pragma unroll
      for (int q = q0; q < q0 + nq; ++q) glds16x4_nt(s0 + q * 4096, fv0, fv0 ^ 16, fv0 ^ 32, fv0 ^ 48, dst + q * 4096);
    };
    u32x4 rb[3][16];
    const unsigned sb0 = lbase + lane * 16, sb1 = sb0 + 65536;
    auto rload = [&](int f) {
      const int t = f >> 1;
      const char* s0 = ((f & 1) ? vg : kg) + (t >> 3) * ITEM + (t & 7) * SLOT;
#pragma unroll
      for (int k = 0; k < 16; ++k)
        rb[((f - 6) >> 1) % 3][k] = __builtin_nontemporal_load((const u32x4*)(s0 + k * 1024 + (fv0 ^ (16 * (k & 3)))));
    };
    auto rstore = [&](int f) {
      const int so = ((f & 1) * 3 + (f >> 1) % 3) * SLOT;
#pragma unroll
      for (int k = 0; k < 16; ++k)
        asm volatile("ds_write_b128 %0, %1 offset:%2" :: "v"(so >= 65536 ? sb1 : sb0), "v"(rb[((f - 6) >> 1) % 3][k]),
                     "i"((so & 65535) + k * 1024) : "memory");
    };
    if (wid == 6) {
      const unsigned w0 = lrow * (KO * 2) + (((lane & 15) ^ (lrow << 2)) << 4);
      const unsigned w1 = (w0 ^ 16) + 4 * (KO * 2) - 1024, w2 = (w0 ^ 32) + 8 * (KO * 2) - 2048, w3 = (w0 ^ 48) + 12 * (KO * 2) - 3072;
      auto wqfill = [&](int c) {
        const char* s0 = wqg + (size_t)c * (32 * KO * 2);
        const unsigned dst = lbase + WQB + (c % NWQ) * 8192;
        glds16x4(s0, w0, w1, w2, w3, dst);
        glds16x4(s0 + 16 * KO * 2, w0, w1, w2, w3, dst + 4096);
      };
#pragma unroll
      for (int c = 0; c < NWQ; ++c) wqfill(c);
      wait_vm<32>();
      bar();
#pragma unroll
      for (int c = 0; c < 16; ++c) {
        if (c < 11) wait_vm<24>(); else if (c == 11) wait_vm<40>(); else if (c == 12) wait_vm<32>();
        else if (c == 13) wait_vm<40>(); else if (c == 14) wait_vm<32>();
        bar();
        if (c + NWQ < 16) wqfill(c + NWQ);
        if (c == 10) rload(7);
        if (c == 12) rload(9);
        if (c == 14) rload(11);
      }
      bar(); bar();
      bar();
#pragma unroll
      for (int T = 0; T < 15; ++T) {
        bar();
        if (T > 0 && T < 11) { rstore(2 * T + 5); rload(2 * T + 11); }
        else if (T >= 12) rstore(2 * T + 3);
        if (T == 7) bar();
        if (T == 10 && sb0 < lbase + 16 * NREP)
          __hip_atomic_store(flags + ((br * 8 + b0) * NREP) * 32 + h + 2 * (sb0 - lbase), (u64)__builtin_amdgcn_s_memrealtime(),
                             __ATOMIC_RELAXED, __HIP_MEMORY_SCOPE_AGENT);
      }
      bar();
      bar();
    } else {
      kvfill(0, 0, 4); kvfill(2, 0, 4);
      bar();
      kvfill(1, 0, 4);
#pragma unroll
      for (int c = 0; c < 16; ++c) {
        bar();
        if (c == 3) { wait_vm<47>(); kvfill(4, 0, 4); }
        if (c == 5) { wait_vm<47>(); kvfill(3, 0, 4); }
        if (c == 7) { wait_vm<47>(); kvfill(5, 0, 4); }
        if (c == 9) { wait_vm<47>(); rload(6); }
        if (c == 11) { wait_vm<47>(); rload(8); }
        if (c == 13) { wait_vm<47>(); rload(10); }
      }
      bar(); bar();
      bar();
      u32x4 pft;
#pragma unroll
      for (int T = 0; T < 15; ++T) {
        bar();
        if (T == 0) { rstore(6); rload(12); }
        if (T < 9) { rstore(2 * T + 8); rload(2 * T + 14); }
        else if (T >= 10 && T < 13) rstore(2 * T + 6);
        if (T == 7) bar();
        if (T == PFT || T == PFT + 1) {
          const char* pw = (const char*)(br ? woB : woA) + (size_t)(128 * rr + 64 * (T - PFT)) * 1024 + 512 * (xcd & 1);
          const unsigned pv = (lane >> 5) * 1024 + 16 * (lane & 31);
          if (T == PFT) asm volatile("global_load_dwordx4 %0, %1, %2" : "=&v"(pft) : "v"(pv), "s"(pw) : "memory");
#pragma unroll
          for (int k = 0; k < 16; ++k)
            asm volatile("global_load_dwordx4 %0, %1, %2\n\tglobal_load_dwordx4 %0, %1, %2 offset:2048"
                         : "+v"(pft) : "v"(pv), "s"(pw + k * 4096) : "memory");
        }
      }
      bar();
      bar();
      asm volatile("s_waitcnt vmcnt(0)" :: "v"(pft) : "memory");
    }
  } else {

  const int qb = wid % 3, kh = wid / 3;
  const unsigned blk = (lane >> 4) & 1, qq = (lane & 15) >> 2, pp = lane & 3;
  int lane2;
  bf16x8 qa[8];
  {
    const char* xp = (const char*)((br ? xB : xA) + (size_t)b0 * SQ * DM) + (size_t)(32 * wid + (lane >> 3)) * (DM * 2) + 16 * (lane & 7);
    u32x4 xr[8][4];
    auto xload = [&](int j) {
#pragma unroll
      for (int i = 0; i < 4; ++i) xr[j][i] = *(const u32x4*)(xp + i * (8 * DM * 2) + j * 128);
    };
#pragma unroll
    for (int j = 0; j < 4; ++j) xload(j);
    char* xs = smem + XSB + wid * 4096;
    const unsigned xw0 = (lane >> 3) * 128 + 16 * ((lane & 7) ^ ((lane >> 4) & 3));
    const unsigned xrd = r32 * 128 + 16 * (hh ^ ((r32 >> 1) & 7));
    f32x16 qacc[4] = {};
    bf16x8 xf[2][4], wf[2][8];
    auto xstage = [&](int j) {
#pragma unroll
      for (int i = 0; i < 4; ++i) *(u32x4*)(xs + i * 1024 + (xw0 ^ (64 * (i & 1)))) = xr[j][i];
      if (j == 0) __builtin_amdgcn_sched_barrier(0);
      if (j + 4 < 8) xload(j + 4);
#pragma unroll
      for (int s = 0; s < 4; ++s) xf[j & 1][s] = *(const bf16x8*)(xs + (xrd ^ (32 * s)));
    };
    const unsigned pp2 = ((pp & 1) << 1) | (pp >> 1);
    const unsigned a0 = lbase + WQB + off_b(8 * hh + qq, 2 * blk + (pp2 >> 1)) + 8 * (pp2 & 1);
    const unsigned a1 = lbase + WQB + off_b(8 * hh + 4 + qq, 2 * blk + (pp2 >> 1)) + 8 * (pp2 & 1);
    auto wread = [&](int c) {
#pragma unroll
      for (int s = 0; s < 2; ++s)
#pragma unroll
        for (int db = 0; db < 4; ++db) {
          const s16x4 lo = vtr((a0 ^ (64 * db)) + (c % NWQ) * 8192 + 4096 * s);
          const s16x4 hi = vtr((a1 ^ (64 * db)) + (c % NWQ) * 8192 + 4096 * s);
          wf[c & 1][4 * s + db] = PK8(lo, hi);
        }
    };
    xstage(0);
    bar();
    wread(0);
#pragma unroll
    for (int c = 0; c < 16; ++c) {
      bar();
      if (c + 1 < 16) wread(c + 1);
      if ((c & 1) && c < 15) xstage((c + 1) / 2);
      __builtin_amdgcn_sched_barrier(0);
#pragma unroll
      for (int s = 0; s < 2; ++s)
#pragma unroll
        for (int db = 0; db < 4; ++db) qacc[db] = MFMA(wf[c & 1][4 * s + db], xf[(c >> 1) & 1][2 * (c & 1) + s], qacc[db]);
      __builtin_amdgcn_sched_barrier(0);
    }
    bar();
    char* ex = smem + WQB;
    asm volatile("v_mbcnt_lo_u32_b32 %0, -1, 0\n\tv_mbcnt_hi_u32_b32 %0, -1, %0" : "=v"(lane2));
#pragma unroll
    for (int db = 0; db < 4; ++db)
#pragma unroll
      for (int s = 0; s < 2; ++s) {
        const f32x16& a = qacc[db];
        const u32x4 w = {cvtpk(a[8 * s + 0], a[8 * s + 1]), cvtpk(a[8 * s + 2], a[8 * s + 3]),
                         cvtpk(a[8 * s + 4], a[8 * s + 5]), cvtpk(a[8 * s + 6], a[8 * s + 7])};
        *(u32x4*)(ex + wid * 8192 + (2 * db + s) * 1024 + lane2 * 16) = w;
      }
    bar();
#pragma unroll
    for (int f = 0; f < 8; ++f) qa[f] = *(const bf16x8*)(ex + qb * 8192 + f * 1024 + lane2 * 16);
  }

  {
  const int lane = lane2, r32 = lane & 31, hh = lane >> 5;
  const unsigned blk = (lane >> 4) & 1, qq = (lane & 15) >> 2, pp = lane & 3;
  constexpr float C = 0.08838834764831845f * 1.4426950408889634f;
  constexpr float TAU = 8.0f / C;
  const unsigned krow = 32 * kh + r32;
  const unsigned kb0 = lbase + krow * 256 + 16 * (hh ^ swz_b(krow));
  const unsigned vrow = 32 * kh + 4 * hh + qq;
  const unsigned vb0 = lbase + 3 * SLOT + vrow * 256 + 16 * ((2 * blk + (pp >> 1)) ^ swz_b(vrow)) + 8 * (pp & 1);
  const unsigned vb1 = lbase + 3 * SLOT + (vrow + 8) * 256 + 16 * ((2 * blk + (pp >> 1)) ^ swz_b(vrow + 8)) + 8 * (pp & 1);

  float m = -INFINITY, l = 0.f;
  f32x16 o[4] = {};
  bf16x8 pf[2];

  auto readk = [&](bf16x8 (&kf)[8], int t) {
#pragma unroll
    for (int ds = 0; ds < 8; ++ds) kf[ds] = ldsr128((kb0 ^ (32 * ds)) + (t % 3) * SLOT);
  };
  auto readv = [&](bf16x8 (&vf)[8], int t) {
#pragma unroll
    for (int ks = 0; ks < 2; ++ks)
#pragma unroll
      for (int d = 0; d < 4; ++d) {
        const s16x4 lo = vtr((vb0 ^ (64 * d)) + (t % 3) * SLOT + 4096 * ks);
        const s16x4 hi = vtr((vb1 ^ (64 * d)) + (t % 3) * SLOT + 4096 * ks);
        vf[4 * ks + d] = PK8(lo, hi);
      }
  };
  auto pv = [&](const bf16x8 (&vf)[8]) {
#pragma unroll
    for (int ks = 0; ks < 2; ++ks)
#pragma unroll
      for (int d = 0; d < 4; ++d) o[d] = MFMA(vf[4 * ks + d], pf[ks], o[d]);
  };
  auto softmax = [&](f32x16& s, float& alpha) -> bool {
#define MX3(a, b, c) __builtin_elementwise_maximum(__builtin_elementwise_maximum(a, b), c)
    float mx = MX3(MX3(MX3(s[0], s[1], s[2]), MX3(s[3], s[4], s[5]), MX3(s[6], s[7], s[8])),
                   MX3(MX3(s[9], s[10], s[11]), MX3(s[12], s[13], s[14]), s[15]), m);
    {
      auto rr = __builtin_amdgcn_permlane32_swap(__float_as_uint(mx), __float_as_uint(mx), false, false);
      mx = __builtin_elementwise_maximum(__uint_as_float(rr[0]), __uint_as_float(rr[1]));
    }
    const bool need = __builtin_amdgcn_ballot_w64(!(mx <= m + TAU)) != 0;
    const float mn = need ? mx : m;
    alpha = __builtin_amdgcn_exp2f((m - mn) * C);
    m = mn;
    const float mc = -mn * C;
    f32x2_t ps[4];
#pragma unroll
    for (int r = 0; r < 8; ++r) {
      const f32x2_t t = __builtin_elementwise_fma((f32x2_t){s[2 * r], s[2 * r + 1]}, (f32x2_t){C, C}, (f32x2_t){mc, mc});
      s[2 * r] = __builtin_amdgcn_exp2f(t[0]); s[2 * r + 1] = __builtin_amdgcn_exp2f(t[1]);
      if (r < 4) ps[r] = (f32x2_t){s[2 * r], s[2 * r + 1]}; else ps[r - 4] += (f32x2_t){s[2 * r], s[2 * r + 1]};
    }
    const f32x2_t pt = (ps[0] + ps[1]) + (ps[2] + ps[3]);
    l = l * alpha + (pt[0] + pt[1]);
    asm volatile("" : "+v"(l));
#pragma unroll
    for (int ks = 0; ks < 2; ++ks) {
      u32x4 w = {cvtpk(s[8 * ks + 0], s[8 * ks + 1]), cvtpk(s[8 * ks + 2], s[8 * ks + 3]),
                 cvtpk(s[8 * ks + 4], s[8 * ks + 5]), cvtpk(s[8 * ks + 6], s[8 * ks + 7])};
      pf[ks] = __builtin_bit_cast(bf16x8, w);
    }
    return need;
  };
  auto finish = [&](const int it, const float mm, float ll) {
    char* ex = smem + WQB;
    ll = swap32_sum(ll);
#pragma unroll
    for (int dd = 0; dd < 2; ++dd)
#pragma unroll
      for (int g = 0; g < 4; ++g) {
        const f32x16& a = kh ? o[dd] : o[2 + dd];
        *(f32x4*)(ex + wid * 8192 + (dd * 4 + g) * 1024 + lane * 16) = (f32x4){a[4 * g], a[4 * g + 1], a[4 * g + 2], a[4 * g + 3]};
      }
    *(f32x2_t*)(ex + 49152 + wid * 512 + lane * 8) = (f32x2_t){mm, ll};
    bar();
    const int pw = wid < 3 ? wid + 3 : wid - 3;
    const f32x2_t ml = *(const f32x2_t*)(ex + 49152 + pw * 512 + lane * 8);
    const float M = fmaxf(mm, ml[0]);
    const float e0 = __builtin_amdgcn_exp2f((mm - M) * C), e1 = __builtin_amdgcn_exp2f((ml[0] - M) * C);
    const float inv = 1.f / (ll * e0 + ml[1] * e1);
    const float f0 = e0 * inv, f1 = e1 * inv;
    const int rbb = 2 * qb + (r32 >> 4), half = rbb >= 3;
    u16* dst = ows + ((((size_t)(br * 16 + 2 * (b0 + it) + half) * NH + h) * 4 + 2 * kh) * 3 + (rbb - 3 * half)) * 512 +
               (hh * 16 + (r32 & 15)) * 8;
#pragma unroll
    for (int dd = 0; dd < 2; ++dd) {
      const f32x16& a = kh ? o[2 + dd] : o[dd];
      unsigned pk[4][2];
#pragma unroll
      for (int g = 0; g < 4; ++g) {
        const f32x4 p = *(const f32x4*)(ex + pw * 8192 + (dd * 4 + g) * 1024 + lane * 16);
        pk[g][0] = cvtpk(a[4 * g] * f0 + p[0] * f1, a[4 * g + 1] * f0 + p[1] * f1);
        pk[g][1] = cvtpk(a[4 * g + 2] * f0 + p[2] * f1, a[4 * g + 3] * f0 + p[3] * f1);
      }
#pragma unroll
      for (int gp = 0; gp < 2; ++gp) {
        auto sx = __builtin_amdgcn_permlane32_swap(pk[2 * gp][0], pk[2 * gp + 1][0], false, false);
        auto sy = __builtin_amdgcn_permlane32_swap(pk[2 * gp][1], pk[2 * gp + 1][1], false, false);
        const u32x4 ov = {sx[0], sy[0], sx[1], sy[1]};
        asm volatile("global_store_dwordx4 %0, %1, off sc1" :: "v"(dst + 1536 * dd + 256 * gp), "v"(ov) : "memory");
      }
    }
  };

  bar();
  f32x16 sn = {};
  {
    bf16x8 kf[8], kg[8]; readk(kf, 0); readk(kg, 1);
    f32x16 s = {};
#pragma unroll
    for (int ds = 0; ds < 8; ++ds) s = MFMA(kf[ds], qa[ds], s);
#pragma unroll
    for (int ds = 0; ds < 8; ++ds) sn = MFMA(kg[ds], qa[ds], sn);
    float alpha; softmax(s, alpha);
  }
#pragma unroll
  for (int T = 0; T < 16; ++T) {
    if (T == 10) wait_vm<0>();
    bar();
    bf16x8 kf[8], vf[8];
    readv(vf, T);
    if (T == 6) {
#pragma unroll
      for (int f = 0; f < 8; ++f) qa[f] = *(const bf16x8*)(smem + WQB + (qb + 3) * 8192 + f * 1024 + lane * 16);
    }
    if (T < 14) readk(kf, T + 2);
    f32x16 s = sn;
    const float mo = m, lo = l;
    if (T == 7) { m = -INFINITY; l = 0.f; }
    if (T < 14) {
      sn = (f32x16){};
#pragma unroll
      for (int i = 0; i < 8; ++i) {
        o[i & 3] = MFMA(vf[i], pf[i >> 2], o[i & 3]);
        if (i >= 2) sn = MFMA(kf[i - 2], qa[i - 2], sn);
      }
      sn = MFMA(kf[6], qa[6], sn); sn = MFMA(kf[7], qa[7], sn);
    } else pv(vf);
    float alpha = 1.f;
    bool need = false;
    if (T < 15) need = softmax(s, alpha);
    if (T < 14) {
      __builtin_amdgcn_sched_group_barrier(0x100, 24, 0);
      if (T == 6) __builtin_amdgcn_sched_group_barrier(0x100, 8, 0);
      __builtin_amdgcn_sched_group_barrier(0x002, 12, 0);
#pragma unroll
      for (int i = 0; i < 3; ++i) {
        __builtin_amdgcn_sched_group_barrier(0x008, 1, 0);
        __builtin_amdgcn_sched_group_barrier(0x002, 4, 0);
      }
#pragma unroll
      for (int i = 3; i < 16; ++i) {
        __builtin_amdgcn_sched_group_barrier(0x008, 1, 0);
        __builtin_amdgcn_sched_group_barrier(0x002, 3, 0);
        __builtin_amdgcn_sched_group_barrier(0x400, 2, 0);
      }
    }
    __builtin_amdgcn_sched_barrier(0);
    if (T == 7) {
      finish(0, mo, lo);
#pragma unroll
      for (int d = 0; d < 4; ++d) o[d] = (f32x16){};
    } else if (need) {
#pragma unroll
      for (int d = 0; d < 4; ++d)
#pragma unroll
        for (int r = 0; r < 16; ++r) o[d][r] *= alpha;
    }
  }
  finish(1, m, l);
  wait_vm<0>();
  }
  }
  bar();
  }

  int lane;
  asm volatile("v_mbcnt_lo_u32_b32 %0, -1, 0\n\tv_mbcnt_hi_u32_b32 %0, -1, %0" : "=v"(lane));
  int wid = wid0, bid = bid0;
  asm volatile("" : "+s"(wid), "+s"(bid));
  if (wid == 0 && lane < NREP)
    __hip_atomic_store(flags + ((((item >> 2) & 1) * 8 + 2 * ((item >> 3) & 3) + 1) * NREP + lane) * 32 + (item & 3) * 8 + (item >> 5),
                       (u64)__builtin_amdgcn_s_memrealtime(), __ATOMIC_RELAXED, __HIP_MEMORY_SCOPE_AGENT);
  const int tid = wid * 64 + lane;
  const unsigned lb = lbase + wid * (ONS * 4096);
  const unsigned bv0 = (lane >> 3) * 1024 + 16 * ((lane & 7) ^ ((lane >> 3) & 2));
  const unsigned bv1 = (bv0 ^ 64) + 7168, bv2 = bv0 + 2 * 7168, bv3 = (bv0 ^ 64) + 3 * 7168;
  const unsigned g = lane >> 4, q = (lane >> 2) & 3;
  const unsigned t0 = lb + (8 * g + q) * 128 + 32 * ((q >> 1) | ((g & 1) << 1)) + 8 * (lane & 3);
  const int tx = bid & 7, tr = bid >> 3;
  const int tb = tx >> 2, mt = 8 * ((tx >> 1) & 1) + (tr & 7), nt = 4 * (tx & 1) + (tr >> 3);
  const char* ag = (const char*)ows + (size_t)((tb * 16 + mt) * NH + 4 * wid) * 12288;
  const char* bg = (const char*)((tb ? woB : woA) + (size_t)(512 * wid) * DM + nt * 64);
  auto fillb = [&](int s) { glds16x4(bg + (size_t)s * 32768, bv0, bv1, bv2, bv3, lb + (s % ONS) * 4096); };
  bf16x8 af[OPA][3], wf[2][4];
  auto loada = [&](int s) {
    asm volatile("global_load_dwordx4 %0, %3, %4\n\tglobal_load_dwordx4 %1, %3, %4 offset:1024\n\tglobal_load_dwordx4 %2, %3, %4 offset:2048"
                 : "=&v"(af[s % OPA][0]), "=&v"(af[s % OPA][1]), "=&v"(af[s % OPA][2]) : "v"(lane * 16), "s"(ag + s * 3072) : "memory");
  };
  auto rdb = [&](int s) {
#pragma unroll
    for (int nb = 0; nb < 4; ++nb) {
      const s16x4 lo = vtr((t0 ^ (32 * nb)) + (s % ONS) * 4096), hi = vtr((t0 ^ (32 * nb)) + (s % ONS) * 4096 + 512);
      wf[s & 1][nb] = PK8(lo, hi);
    }
  };
#pragma unroll
  for (int c = 0; c < ONS; ++c) fillb(c);
  int miss = -1;
  {
    const u64* fp = flags + ((tb * 8 + (mt >> 1)) * NREP + (nt & (NREP - 1))) * 32 + 4 * wid + (lane & 3);
    const u64 tp = __builtin_amdgcn_s_memrealtime();
    for (;;) {
      const u64 f = __hip_atomic_load(fp, __ATOMIC_RELAXED, __HIP_MEMORY_SCOPE_AGENT);
      const u64 now = __builtin_amdgcn_s_memrealtime();
      const u64 bad = __builtin_amdgcn_ballot_w64(!(f - tstart <= now - tstart + SLACK));
      if (bad == 0) break;
      if (now - tp > patience) { miss = 4 * wid + (__builtin_ctzll(bad) & 3); break; }
      __builtin_amdgcn_s_sleep(1);
    }
  }
  f32x4 acc[3][4] = {};
  if (miss < 0) {
#pragma unroll
    for (int c = 0; c < OPA; ++c) loada(c);
    wait_vm<4 * (ONS - 1) + 3 * OPA>();
    rdb(0);
#pragma unroll
    for (int s = 0; s < 16; ++s) {
      asm volatile("s_waitcnt vmcnt(%0) lgkmcnt(0)" :: "i"(oproj_wait(s)) : "memory");
      if (s < 15) {
        if (s + ONS < 16) fillb(s + ONS);
        rdb(s + 1);
      }
      asm volatile("" : "+v"(af[s % OPA][0]), "+v"(af[s % OPA][1]), "+v"(af[s % OPA][2]));
#pragma unroll
      for (int i = 0; i < 3; ++i)
#pragma unroll
        for (int nb = 0; nb < 4; ++nb) acc[i][nb] = MFMA16(wf[s & 1][nb], af[s % OPA][i], acc[i][nb]);
      __builtin_amdgcn_sched_barrier(0);
      if (s + OPA < 16) loada(s + OPA);
      __builtin_amdgcn_sched_barrier(0);
    }
  } else wait_vm<0>();
  bar();
#pragma unroll
  for (int i = 0; i < 3; ++i)
#pragma unroll
    for (int nb = 0; nb < 4; ++nb) *(f32x4*)(smem + wid * 12288 + (i * 4 + nb) * 1024 + lane * 16) = acc[i][nb];
  if (lane == 0) lds_st(lbase + 98304 + 4 * wid, (unsigned)miss);
  bar();
  int mh = (int)lds_ld(lbase + 98304 + 4 * (lane & 7));
  {
    const u64 mb = __builtin_amdgcn_ballot_w64(mh >= 0);
    mh = mb ? __builtin_amdgcn_readlane(mh, __builtin_ctzll(mb)) : -1;
  }
  if (mh < 0) {
    if (tid < 384) {
      const int row = tid >> 3, c8 = tid & 7;
      const char* p = smem + ((row >> 4) * 4 + (c8 >> 1)) * 1024 + ((c8 & 1) * 32 + (row & 15)) * 16;
      f32x4 s0 = *(const f32x4*)p, s1 = *(const f32x4*)(p + 256);
#pragma unroll
      for (int w = 1; w < 8; ++w) { s0 += *(const f32x4*)(p + w * 12288); s1 += *(const f32x4*)(p + w * 12288 + 256); }
      *(u32x4*)(out + (size_t)(mt * 48 + row) * (2 * DM) + tb * DM + nt * 64 + 8 * c8) =
          (u32x4){cvtpk(s0[0], s0[1]), cvtpk(s0[2], s0[3]), cvtpk(s1[0], s1[1]), cvtpk(s1[2], s1[3])};
    }
    return;
  }
  item = (((mh & 7) * 4 + (mt >> 2)) << 3) + tb * 4 + (mh >> 3);
  patience = TMO1;
  bar();
  }
}

extern "C" void kernel_launch(void* const* d_in, const int* in_sizes, int n_in,
                              void* d_out, int out_size, void* d_ws, size_t ws_size,
                              hipStream_t stream) {
  const u16* xA = (const u16*)d_in[0]; const u16* kA = (const u16*)d_in[1]; const u16* vA = (const u16*)d_in[2];
  const u16* wqA = (const u16*)d_in[3]; const u16* woA = (const u16*)d_in[4];
  const u16* xB = (const u16*)d_in[5]; const u16* kB = (const u16*)d_in[6]; const u16* vB = (const u16*)d_in[7];
  const u16* wqB = (const u16*)d_in[8]; const u16* woB = (const u16*)d_in[9];
  u16* ows = (u16*)d_ws;
  attn_kernel<<<256, 512, 0, stream>>>(xA, kA, vA, wqA, xB, kB, vB, wqB, ows, woA, woB, (u16*)d_out);
}
```

```cpp
#include <hip/hip_runtime.h>
#include <hip/hip_bf16.h>
#include <stdint.h>

typedef unsigned short u16;
using bf16x8 = __attribute__((ext_vector_type(8))) short;
using s16x4  = __attribute__((ext_vector_type(4))) short;
using f32x16 = __attribute__((ext_vector_type(16))) float;
using f32x4  = __attribute__((ext_vector_type(4))) float;
using u32x4  = __attribute__((ext_vector_type(4))) unsigned;
using u32x2  = __attribute__((ext_vector_type(2))) unsigned;
typedef short v4i16_t __attribute__((ext_vector_type(4)));
typedef __attribute__((address_space(3))) char* lds_ptr;

constexpr int NB = 8, SQ = 96, DM = 512, NH = 32, SK = 512, DH = 128;
constexpr int MROWS = NB * SQ;
constexpr int KO = NH * DH;

__device__ __forceinline__ unsigned swz_b(unsigned row) { return ((row & 3) << 2) | ((row >> 2) & 3); }
__device__ __forceinline__ unsigned off_b(unsigned row, unsigned ch) { return 256u * row + 16u * (ch ^ swz_b(row)); }

__device__ __forceinline__ void glds16(const void* g, unsigned lds_dst) {
  unsigned keep;
  asm volatile("s_mov_b32 %0, m0\n\ts_mov_b32 m0, %2\n\ts_nop 0\n\tglobal_load_lds_dwordx4 %1, off\n\ts_mov_b32 m0, %0"
               : "=&s"(keep) : "v"(g), "s"(lds_dst) : "memory");
}
__device__ __forceinline__ void glds16_nt(const void* g, unsigned lds_dst) {
  unsigned keep;
  asm volatile("s_mov_b32 %0, m0\n\ts_mov_b32 m0, %2\n\ts_nop 0\n\tglobal_load_lds_dwordx4 %1, off nt\n\ts_mov_b32 m0, %0"
               : "=&s"(keep) : "v"(g), "s"(lds_dst) : "memory");
}
__device__ __forceinline__ void glds16x4_nt(const void* sbase, unsigned v0, unsigned v1, unsigned v2, unsigned v3, unsigned lds_dst) {
  unsigned keep;
  asm volatile("s_mov_b32 %0, m0\n\ts_mov_b32 m0, %6\n\ts_nop 0\n\t"
               "global_load_lds_dwordx4 %1, %5 nt\n\t"
               "global_load_lds_dwordx4 %2, %5 offset:1024 nt\n\t"
               "global_load_lds_dwordx4 %3, %5 offset:2048 nt\n\t"
               "global_load_lds_dwordx4 %4, %5 offset:3072 nt\n\t"
               "s_mov_b32 m0, %0"
               : "=&s"(keep) : "v"(v0), "v"(v1), "v"(v2), "v"(v3), "s"(sbase), "s"(lds_dst) : "memory");
}
template <int N> __device__ __forceinline__ void wait_vm() { asm volatile("s_waitcnt vmcnt(%0)" :: "i"(N) : "memory"); }
__device__ __forceinline__ void bar() { asm volatile("s_waitcnt lgkmcnt(0)\n\ts_barrier" ::: "memory"); }

__device__ __forceinline__ s16x4 vtr(unsigned a) {
  return __builtin_bit_cast(s16x4, __builtin_amdgcn_ds_read_tr16_b64_v4i16((__attribute__((address_space(3))) v4i16_t*)(uintptr_t)a));
}
__device__ __forceinline__ bf16x8 ldsr128(unsigned a) {
  return *(const __attribute__((address_space(3))) bf16x8*)(uintptr_t)a;
}
typedef __bf16 bf16x2_t __attribute__((ext_vector_type(2)));
typedef float f32x2_t __attribute__((ext_vector_type(2)));
__device__ __forceinline__ unsigned cvtpk(float lo, float hi) {
  return __builtin_bit_cast(unsigned, __builtin_convertvector((f32x2_t){lo, hi}, bf16x2_t));
}
__device__ __forceinline__ float swap32_max(float v) {
  auto rr = __builtin_amdgcn_permlane32_swap(__float_as_uint(v), __float_as_uint(v), false, false);
  return fmaxf(__uint_as_float(rr[0]), __uint_as_float(rr[1]));
}
__device__ __forceinline__ float swap32_sum(float v) {
  auto rr = __builtin_amdgcn_permlane32_swap(__float_as_uint(v), __float_as_uint(v), false, false);
  return __uint_as_float(rr[0]) + __uint_as_float(rr[1]);
}
#define MFMA(a, b, c) __builtin_amdgcn_mfma_f32_32x32x16_bf16(a, b, c, 0, 0, 0)
#define PK8(L, H) (bf16x8){L[0], L[1], L[2], L[3], H[0], H[1], H[2], H[3]}

constexpr int PFT = 12;
constexpr int SLOT = 16384, WQB = 6 * SLOT;
constexpr int NWQ = 5, XSB = WQB + NWQ * 8192;

__device__ __forceinline__ void glds16x4(const void* sbase, unsigned v0, unsigned v1, unsigned v2, unsigned v3, unsigned lds_dst) {
  unsigned keep;
  asm volatile("s_mov_b32 %0, m0\n\ts_mov_b32 m0, %6\n\ts_nop 0\n\t"
               "global_load_lds_dwordx4 %1, %5\n\t"
               "global_load_lds_dwordx4 %2, %5 offset:1024\n\t"
               "global_load_lds_dwordx4 %3, %5 offset:2048\n\t"
               "global_load_lds_dwordx4 %4, %5 offset:3072\n\t"
               "s_mov_b32 m0, %0"
               : "=&s"(keep) : "v"(v0), "v"(v1), "v"(v2), "v"(v3), "s"(sbase), "s"(lds_dst) : "memory");
}


constexpr int ONS = 5, OPA = 8;
#define MFMA16(a, b, c) __builtin_amdgcn_mfma_f32_16x16x32_bf16(a, b, c, 0, 0, 0)
constexpr int oproj_wait(int s) {
  int nw = 0, na = 0; bool sw = false, sa = false;
  for (int c = 0; c < ONS; ++c) { if (sw) nw += 4; if (c == s + 1) sw = true; }
  for (int c = 0; c < OPA; ++c) { if (sw) nw += 3; if (sa) na += 3; if (c == s) sa = true; }
  for (int t = 0; t < s; ++t) {
    if (t + ONS < 16) { if (sw) nw += 4; if (sa) na += 4; if (t + ONS == s + 1) sw = true; }
    if (t + OPA < 16) { if (sw) nw += 3; if (sa) na += 3; if (t + OPA == s) sa = true; }
  }
  if (s + 1 >= 16) return na;
  return nw < na ? nw : na;
}
constexpr size_t FLAG_OFF = 16u << 20;
constexpr int NREP = 4;
constexpr unsigned TMO0 = 30000, TMO1 = 500, SLACK = 200;
typedef unsigned long long u64;
typedef volatile __attribute__((address_space(3))) unsigned* lds_word;
__device__ __forceinline__ void lds_st(unsigned a, unsigned v) { *(lds_word)(uintptr_t)a = v; }
__device__ __forceinline__ unsigned lds_ld(unsigned a) { return *(lds_word)(uintptr_t)a; }

__global__ __launch_bounds__(512, 2) void attn_kernel(
    const u16* __restrict__ xA, const u16* __restrict__ kA, const u16* __restrict__ vA, const u16* __restrict__ wqA,
    const u16* __restrict__ xB, const u16* __restrict__ kB, const u16* __restrict__ vB, const u16* __restrict__ wqB,
    u16* __restrict__ ows, const u16* __restrict__ woA, const u16* __restrict__ woB, u16* __restrict__ out) {
  __shared__ __attribute__((aligned(16))) char smem[WQB + 65536];
  const u64 tstart = __builtin_amdgcn_s_memrealtime();
  const int wid0 = __builtin_amdgcn_readfirstlane(threadIdx.x >> 6);
  const int bid0 = blockIdx.x;
  const unsigned lbase = (unsigned)(uintptr_t)(lds_ptr)smem;
  u64* const flags = (u64*)((char*)ows + FLAG_OFF);
  int item = bid0;
  unsigned patience = TMO0;
  for (;;) {
  {
  int lane;
  asm volatile("v_mbcnt_lo_u32_b32 %0, -1, 0\n\tv_mbcnt_hi_u32_b32 %0, -1, %0" : "=v"(lane));
  int wid = wid0;
  asm volatile("" : "+s"(wid));
  const int xcd = item & 7, rr = item >> 3;
  const int br = xcd >> 2, h = (xcd & 3) * 8 + (rr >> 2), b0 = 2 * (rr & 3);
  const int r32 = lane & 31, hh = lane >> 5;

  if (wid >= 6) {
    const char* kg  = (const char*)((br ? kB : kA) + (size_t)(b0 * NH + h) * SK * DH);
    const char* vg  = (const char*)((br ? vB : vA) + (size_t)(b0 * NH + h) * SK * DH);
    const char* wqg = (const char*)((br ? wqB : wqA) + h * DH);
    constexpr size_t ITEM = (size_t)NH * SK * DH * 2;
    const unsigned lrow = lane >> 4;
    const unsigned fv0 = lrow * 256 + (((lane & 15) ^ (lrow << 2)) << 4);
    auto kvfill = [&](int f, int q0, int nq) {
      const int t = f >> 1;
      const char* s0 = ((f & 1) ? vg : kg) + (t >> 3) * ITEM + (t & 7) * SLOT;
      const unsigned dst = lbase + ((f & 1) * 3 + t % 3) * SLOT;
#pragma unroll
      for (int q = q0; q < q0 + nq; ++q) glds16x4_nt(s0 + q * 4096, fv0, fv0 ^ 16, fv0 ^ 32, fv0 ^ 48, dst + q * 4096);
    };
    u32x4 rb[3][16];
    const unsigned sb0 = lbase + lane * 16, sb1 = sb0 + 65536;
    auto rload = [&](int f) {
      const int t = f >> 1;
      const char* s0 = ((f & 1) ? vg : kg) + (t >> 3) * ITEM + (t & 7) * SLOT;
#pragma unroll
      for (int k = 0; k < 16; ++k)
        rb[((f - 6) >> 1) % 3][k] = __builtin_nontemporal_load((const u32x4*)(s0 + k * 1024 + (fv0 ^ (16 * (k & 3)))));
    };
    auto rstore = [&](int f) {
      const int so = ((f & 1) * 3 + (f >> 1) % 3) * SLOT;
#pragma unroll
      for (int k = 0; k < 16; ++k)
        asm volatile("ds_write_b128 %0, %1 offset:%2" :: "v"(so >= 65536 ? sb1 : sb0), "v"(rb[((f - 6) >> 1) % 3][k]),
                     "i"((so & 65535) + k * 1024) : "memory");
    };
    if (wid == 6) {
      const unsigned w0 = lrow * (KO * 2) + (((lane & 15) ^ (lrow << 2)) << 4);
      const unsigned w1 = (w0 ^ 16) + 4 * (KO * 2) - 1024, w2 = (w0 ^ 32) + 8 * (KO * 2) - 2048, w3 = (w0 ^ 48) + 12 * (KO * 2) - 3072;
      auto wqfill = [&](int c) {
        const char* s0 = wqg + (size_t)c * (32 * KO * 2);
        const unsigned dst = lbase + WQB + (c % NWQ) * 8192;
        glds16x4(s0, w0, w1, w2, w3, dst);
        glds16x4(s0 + 16 * KO * 2, w0, w1, w2, w3, dst + 4096);
      };
#pragma unroll
      for (int c = 0; c < NWQ; ++c) wqfill(c);
      wait_vm<32>();
      bar();
#pragma unroll
      for (int c = 0; c < 16; ++c) {
        if (c < 11) wait_vm<24>(); else if (c == 11) wait_vm<40>(); else if (c == 12) wait_vm<32>();
        else if (c == 13) wait_vm<40>(); else if (c == 14) wait_vm<32>();
        bar();
        if (c + NWQ < 16) wqfill(c + NWQ);
        if (c == 10) rload(7);
        if (c == 12) rload(9);
        if (c == 14) rload(11);
      }
      bar(); bar();
      bar();
#pragma unroll
      for (int T = 0; T < 15; ++T) {
        bar();
        if (T > 0 && T < 11) { rstore(2 * T + 5); rload(2 * T + 11); }
        else if (T >= 12) rstore(2 * T + 3);
        if (T == 7) bar();
        if (T == 10 && sb0 < lbase + 16 * NREP)
          __hip_atomic_store(flags + ((br * 8 + b0) * NREP) * 32 + h + 2 * (sb0 - lbase), (u64)__builtin_amdgcn_s_memrealtime(),
                             __ATOMIC_RELAXED, __HIP_MEMORY_SCOPE_AGENT);
      }
      bar();
      bar();
    } else {
      kvfill(0, 0, 4); kvfill(2, 0, 4);
      bar();
      kvfill(1, 0, 4);
#pragma unroll
      for (int c = 0; c < 16; ++c) {
        bar();
        if (c == 3) { wait_vm<47>(); kvfill(4, 0, 4); }
        if (c == 5) { wait_vm<47>(); kvfill(3, 0, 4); }
        if (c == 7) { wait_vm<47>(); kvfill(5, 0, 4); }
        if (c == 9) { wait_vm<47>(); rload(6); }
        if (c == 11) { wait_vm<47>(); rload(8); }
        if (c == 13) { wait_vm<47>(); rload(10); }
      }
      bar(); bar();
      bar();
      u32x4 pft;
#pragma unroll
      for (int T = 0; T < 15; ++T) {
        bar();
        if (T == 0) { rstore(6); rload(12); }
        if (T < 9) { rstore(2 * T + 8); rload(2 * T + 14); }
        else if (T >= 10 && T < 13) rstore(2 * T + 6);
        if (T == 7) bar();
        if (T == PFT || T == PFT + 1) {
          const char* pw = (const char*)(br ? woB : woA) + (size_t)(128 * rr + 64 * (T - PFT)) * 1024 + 512 * (xcd & 1);
          const unsigned pv = (lane >> 5) * 1024 + 16 * (lane & 31);
          if (T == PFT) asm volatile("global_load_dwordx4 %0, %1, %2" : "=&v"(pft) : "v"(pv), "s"(pw) : "memory");
#pragma unroll
          for (int k = 0; k < 16; ++k)
            asm volatile("global_load_dwordx4 %0, %1, %2\n\tglobal_load_dwordx4 %0, %1, %2 offset:2048"
                         : "+v"(pft) : "v"(pv), "s"(pw + k * 4096) : "memory");
        }
      }
      bar();
      bar();
      asm volatile("s_waitcnt vmcnt(0)" :: "v"(pft) : "memory");
    }
  } else {

  const int qb = wid % 3, kh = wid / 3;
  const unsigned blk = (lane >> 4) & 1, qq = (lane & 15) >> 2, pp = lane & 3;
  int lane2;
  bf16x8 qa[8];
  {
    const char* xp = (const char*)((br ? xB : xA) + (size_t)b0 * SQ * DM) + (size_t)(32 * wid + (lane >> 3)) * (DM * 2) + 16 * (lane & 7);
    u32x4 xr[8][4];
    auto xload = [&](int j) {
#pragma unroll
      for (int i = 0; i < 4; ++i) xr[j][i] = *(const u32x4*)(xp + i * (8 * DM * 2) + j * 128);
    };
#pragma unroll
    for (int j = 0; j < 4; ++j) xload(j);
    char* xs = smem + XSB + wid * 4096;
    const unsigned xw0 = (lane >> 3) * 128 + 16 * ((lane & 7) ^ ((lane >> 4) & 3));
    const unsigned xrd = r32 * 128 + 16 * (hh ^ ((r32 >> 1) & 7));
    f32x16 qacc[4] = {};
    bf16x8 xf[2][4], wf[2][8];
    auto xstage = [&](int j) {
#pragma unroll
      for (int i = 0; i < 4; ++i) *(u32x4*)(xs + i * 1024 + (xw0 ^ (64 * (i & 1)))) = xr[j][i];
      if (j == 0) __builtin_amdgcn_sched_barrier(0);
      if (j + 4 < 8) xload(j + 4);
#pragma unroll
      for (int s = 0; s < 4; ++s) xf[j & 1][s] = *(const bf16x8*)(xs + (xrd ^ (32 * s)));
    };
    const unsigned pp2 = ((pp & 1) << 1) | (pp >> 1);
    const unsigned a0 = lbase + WQB + off_b(8 * hh + qq, 2 * blk + (pp2 >> 1)) + 8 * (pp2 & 1);
    const unsigned a1 = lbase + WQB + off_b(8 * hh + 4 + qq, 2 * blk + (pp2 >> 1)) + 8 * (pp2 & 1);
    auto wread = [&](int c) {
#pragma unroll
      for (int s = 0; s < 2; ++s)
#pragma unroll
        for (int db = 0; db < 4; ++db) {
          const s16x4 lo = vtr((a0 ^ (64 * db)) + (c % NWQ) * 8192 + 4096 * s);
          const s16x4 hi = vtr((a1 ^ (64 * db)) + (c % NWQ) * 8192 + 4096 * s);
          wf[c & 1][4 * s + db] = PK8(lo, hi);
        }
    };
    xstage(0);
    bar();
    wread(0);
#pragma unroll
    for (int c = 0; c < 16; ++c) {
      bar();
      if (c + 1 < 16) wread(c + 1);
      if ((c & 1) && c < 15) xstage((c + 1) / 2);
      __builtin_amdgcn_sched_barrier(0);
#pragma unroll
      for (int s = 0; s < 2; ++s)
#pragma unroll
        for (int db = 0; db < 4; ++db) qacc[db] = MFMA(wf[c & 1][4 * s + db], xf[(c >> 1) & 1][2 * (c & 1) + s], qacc[db]);
      __builtin_amdgcn_sched_barrier(0);
    }
    bar();
    char* ex = smem + WQB;
    asm volatile("v_mbcnt_lo_u32_b32 %0, -1, 0\n\tv_mbcnt_hi_u32_b32 %0, -1, %0" : "=v"(lane2));
#pragma unroll
    for (int db = 0; db < 4; ++db)
#pragma unroll
      for (int s = 0; s < 2; ++s) {
        const f32x16& a = qacc[db];
        const u32x4 w = {cvtpk(a[8 * s + 0], a[8 * s + 1]), cvtpk(a[8 * s + 2], a[8 * s + 3]),
                         cvtpk(a[8 * s + 4], a[8 * s + 5]), cvtpk(a[8 * s + 6], a[8 * s + 7])};
        *(u32x4*)(ex + wid * 8192 + (2 * db + s) * 1024 + lane2 * 16) = w;
      }
    bar();
#pragma unroll
    for (int f = 0; f < 8; ++f) qa[f] = *(const bf16x8*)(ex + qb * 8192 + f * 1024 + lane2 * 16);
  }

  {
  const int lane = lane2, r32 = lane & 31, hh = lane >> 5;
  const unsigned blk = (lane >> 4) & 1, qq = (lane & 15) >> 2, pp = lane & 3;
  constexpr float C = 0.08838834764831845f * 1.4426950408889634f;
  constexpr float TAU = 8.0f / C;
  const unsigned krow = 32 * kh + r32;
  const unsigned kb0 = lbase + krow * 256 + 16 * (hh ^ swz_b(krow));
  const unsigned vrow = 32 * kh + 4 * hh + qq;
  const unsigned vb0 = lbase + 3 * SLOT + vrow * 256 + 16 * ((2 * blk + (pp >> 1)) ^ swz_b(vrow)) + 8 * (pp & 1);
  const unsigned vb1 = lbase + 3 * SLOT + (vrow + 8) * 256 + 16 * ((2 * blk + (pp >> 1)) ^ swz_b(vrow + 8)) + 8 * (pp & 1);

  float m = -INFINITY, l = 0.f;
  f32x16 o[4] = {};
  bf16x8 pf[2];

  auto readk = [&](bf16x8 (&kf)[8], int t) {
#pragma unroll
    for (int ds = 0; ds < 8; ++ds) kf[ds] = ldsr128((kb0 ^ (32 * ds)) + (t % 3) * SLOT);
  };
  auto readv = [&](bf16x8 (&vf)[8], int t) {
#pragma unroll
    for (int ks = 0; ks < 2; ++ks)
#pragma unroll
      for (int d = 0; d < 4; ++d) {
        const s16x4 lo = vtr((vb0 ^ (64 * d)) + (t % 3) * SLOT + 4096 * ks);
        const s16x4 hi = vtr((vb1 ^ (64 * d)) + (t % 3) * SLOT + 4096 * ks);
        vf[4 * ks + d] = PK8(lo, hi);
      }
  };
  auto pv = [&](const bf16x8 (&vf)[8]) {
#pragma unroll
    for (int ks = 0; ks < 2; ++ks)
#pragma unroll
      for (int d = 0; d < 4; ++d) o[d] = MFMA(vf[4 * ks + d], pf[ks], o[d]);
  };
  auto softmax = [&](f32x16& s, float& alpha) -> bool {
#define MX3(a, b, c) __builtin_elementwise_maximum(__builtin_elementwise_maximum(a, b), c)
    float mx = MX3(MX3(MX3(s[0], s[1], s[2]), MX3(s[3], s[4], s[5]), MX3(s[6], s[7], s[8])),
                   MX3(MX3(s[9], s[10], s[11]), MX3(s[12], s[13], s[14]), s[15]), m);
    {
      auto rr = __builtin_amdgcn_permlane32_swap(__float_as_uint(mx), __float_as_uint(mx), false, false);
      mx = __builtin_elementwise_maximum(__uint_as_float(rr[0]), __uint_as_float(rr[1]));
    }
    const bool need = __builtin_amdgcn_ballot_w64(!(mx <= m + TAU)) != 0;
    const float mn = need ? mx : m;
    alpha = __builtin_amdgcn_exp2f((m - mn) * C);
    m = mn;
    const float mc = -mn * C;
    f32x2_t ps[4];
#pragma unroll
    for (int r = 0; r < 8; ++r) {
      const f32x2_t t = __builtin_elementwise_fma((f32x2_t){s[2 * r], s[2 * r + 1]}, (f32x2_t){C, C}, (f32x2_t){mc, mc});
      s[2 * r] = __builtin_amdgcn_exp2f(t[0]); s[2 * r + 1] = __builtin_amdgcn_exp2f(t[1]);
      if (r < 4) ps[r] = (f32x2_t){s[2 * r], s[2 * r + 1]}; else ps[r - 4] += (f32x2_t){s[2 * r], s[2 * r + 1]};
    }
    const f32x2_t pt = (ps[0] + ps[1]) + (ps[2] + ps[3]);
    l = l * alpha + (pt[0] + pt[1]);
    asm volatile("" : "+v"(l));
#pragma unroll
    for (int ks = 0; ks < 2; ++ks) {
      u32x4 w = {cvtpk(s[8 * ks + 0], s[8 * ks + 1]), cvtpk(s[8 * ks + 2], s[8 * ks + 3]),
                 cvtpk(s[8 * ks + 4], s[8 * ks + 5]), cvtpk(s[8 * ks + 6], s[8 * ks + 7])};
      pf[ks] = __builtin_bit_cast(bf16x8, w);
    }
    return need;
  };
  auto finish = [&](const int it, const float mm, float ll) {
    char* ex = smem + WQB;
    ll = swap32_sum(ll);
#pragma unroll
    for (int dd = 0; dd < 2; ++dd)
#pragma unroll
      for (int g = 0; g < 4; ++g) {
        const f32x16& a = kh ? o[dd] : o[2 + dd];
        *(f32x4*)(ex + wid * 8192 + (dd * 4 + g) * 1024 + lane * 16) = (f32x4){a[4 * g], a[4 * g + 1], a[4 * g + 2], a[4 * g + 3]};
      }
    *(f32x2_t*)(ex + 49152 + wid * 512 + lane * 8) = (f32x2_t){mm, ll};
    bar();
    const int pw = wid < 3 ? wid + 3 : wid - 3;
    const f32x2_t ml = *(const f32x2_t*)(ex + 49152 + pw * 512 + lane * 8);
    const float M = fmaxf(mm, ml[0]);
    const float e0 = __builtin_amdgcn_exp2f((mm - M) * C), e1 = __builtin_amdgcn_exp2f((ml[0] - M) * C);
    const float inv = 1.f / (ll * e0 + ml[1] * e1);
    const float f0 = e0 * inv, f1 = e1 * inv;
    const int rbb = 2 * qb + (r32 >> 4), half = rbb >= 3;
    u16* dst = ows + ((((size_t)(br * 16 + 2 * (b0 + it) + half) * NH + h) * 4 + 2 * kh) * 3 + (rbb - 3 * half)) * 512 +
               (hh * 16 + (r32 & 15)) * 8;
#pragma unroll
    for (int dd = 0; dd < 2; ++dd) {
      const f32x16& a = kh ? o[2 + dd] : o[dd];
      unsigned pk[4][2];
#pragma unroll
      for (int g = 0; g < 4; ++g) {
        const f32x4 p = *(const f32x4*)(ex + pw * 8192 + (dd * 4 + g) * 1024 + lane * 16);
        pk[g][0] = cvtpk(a[4 * g] * f0 + p[0] * f1, a[4 * g + 1] * f0 + p[1] * f1);
        pk[g][1] = cvtpk(a[4 * g + 2] * f0 + p[2] * f1, a[4 * g + 3] * f0 + p[3] * f1);
      }
#pragma unroll
      for (int gp = 0; gp < 2; ++gp) {
        auto sx = __builtin_amdgcn_permlane32_swap(pk[2 * gp][0], pk[2 * gp + 1][0], false, false);
        auto sy = __builtin_amdgcn_permlane32_swap(pk[2 * gp][1], pk[2 * gp + 1][1], false, false);
        const u32x4 ov = {sx[0], sy[0], sx[1], sy[1]};
        asm volatile("global_store_dwordx4 %0, %1, off sc1" :: "v"(dst + 1536 * dd + 256 * gp), "v"(ov) : "memory");
      }
    }
  };

  bar();
  f32x16 sn = {};
  {
    bf16x8 kf[8], kg[8]; readk(kf, 0); readk(kg, 1);
    f32x16 s = {};
#pragma unroll
    for (int ds = 0; ds < 8; ++ds) s = MFMA(kf[ds], qa[ds], s);
#pragma unroll
    for (int ds = 0; ds < 8; ++ds) sn = MFMA(kg[ds], qa[ds], sn);
    float alpha; softmax(s, alpha);
  }
#pragma unroll
  for (int T = 0; T < 16; ++T) {
    if (T == 10) wait_vm<0>();
    bar();
    bf16x8 kf[8], vf[8];
    readv(vf, T);
    if (T == 6) {
#pragma unroll
      for (int f = 0; f < 8; ++f) qa[f] = *(const bf16x8*)(smem + WQB + (qb + 3) * 8192 + f * 1024 + lane * 16);
    }
    if (T < 14) readk(kf, T + 2);
    f32x16 s = sn;
    const float mo = m, lo = l;
    if (T == 7) { m = -INFINITY; l = 0.f; }
    if (T < 14) {
      sn = (f32x16){};
#pragma unroll
      for (int i = 0; i < 8; ++i) {
        o[i & 3] = MFMA(vf[i], pf[i >> 2], o[i & 3]);
        if (i >= 2) sn = MFMA(kf[i - 2], qa[i - 2], sn);
      }
      sn = MFMA(kf[6], qa[6], sn); sn = MFMA(kf[7], qa[7], sn);
    } else pv(vf);
    float alpha = 1.f;
    bool need = false;
    if (T < 15) need = softmax(s, alpha);
    if (T < 14) {
      __builtin_amdgcn_sched_group_barrier(0x100, 24, 0);
      if (T == 6) __builtin_amdgcn_sched_group_barrier(0x100, 8, 0);
      __builtin_amdgcn_sched_group_barrier(0x002, 12, 0);
#pragma unroll
      for (int i = 0; i < 3; ++i) {
        __builtin_amdgcn_sched_group_barrier(0x008, 1, 0);
        __builtin_amdgcn_sched_group_barrier(0x002, 4, 0);
      }
#pragma unroll
      for (int i = 3; i < 16; ++i) {
        __builtin_amdgcn_sched_group_barrier(0x008, 1, 0);
        __builtin_amdgcn_sched_group_barrier(0x002, 3, 0);
        __builtin_amdgcn_sched_group_barrier(0x400, 2, 0);
      }
    }
    __builtin_amdgcn_sched_barrier(0);
    if (T == 7) {
      finish(0, mo, lo);
#pragma unroll
      for (int d = 0; d < 4; ++d) o[d] = (f32x16){};
    } else if (need) {
#pragma unroll
      for (int d = 0; d < 4; ++d)
#pragma unroll
        for (int r = 0; r < 16; ++r) o[d][r] *= alpha;
    }
  }
  finish(1, m, l);
  wait_vm<0>();
  }
  }
  bar();
  }

  int lane;
  asm volatile("v_mbcnt_lo_u32_b32 %0, -1, 0\n\tv_mbcnt_hi_u32_b32 %0, -1, %0" : "=v"(lane));
  int wid = wid0, bid = bid0;
  asm volatile("" : "+s"(wid), "+s"(bid));
  if (wid == 0 && lane < NREP)
    __hip_atomic_store(flags + ((((item >> 2) & 1) * 8 + 2 * ((item >> 3) & 3) + 1) * NREP + lane) * 32 + (item & 3) * 8 + (item >> 5),
                       (u64)__builtin_amdgcn_s_memrealtime(), __ATOMIC_RELAXED, __HIP_MEMORY_SCOPE_AGENT);
  const int tid = wid * 64 + lane;
  const unsigned lb = lbase + wid * (ONS * 4096);
  const unsigned bv0 = (lane >> 3) * 1024 + 16 * ((lane & 7) ^ ((lane >> 3) & 2));
  const unsigned bv1 = (bv0 ^ 64) + 7168, bv2 = bv0 + 2 * 7168, bv3 = (bv0 ^ 64) + 3 * 7168;
  const unsigned g = lane >> 4, q = (lane >> 2) & 3;
  const unsigned t0 = lb + (8 * g + q) * 128 + 32 * ((q >> 1) | ((g & 1) << 1)) + 8 * (lane & 3);
  const int tx = bid & 7, tr = bid >> 3;
  const int tb = tx >> 2, mt = 8 * ((tx >> 1) & 1) + (tr & 7), nt = 4 * (tx & 1) + (tr >> 3);
  const char* ag = (const char*)ows + (size_t)((tb * 16 + mt) * NH + 4 * wid) * 12288;
  const char* bg = (const char*)((tb ? woB : woA) + (size_t)(512 * wid) * DM + nt * 64);
  auto fillb = [&](int s) { glds16x4(bg + (size_t)s * 32768, bv0, bv1, bv2, bv3, lb + (s % ONS) * 4096); };
  bf16x8 af[OPA][3], wf[2][4];
  auto loada = [&](int s) {
    asm volatile("global_load_dwordx4 %0, %3, %4 sc1\n\tglobal_load_dwordx4 %1, %3, %4 offset:1024 sc1\n\tglobal_load_dwordx4 %2, %3, %4 offset:2048 sc1"
                 : "=&v"(af[s % OPA][0]), "=&v"(af[s % OPA][1]), "=&v"(af[s % OPA][2]) : "v"(lane * 16), "s"(ag + s * 3072) : "memory");
  };
  auto rdb = [&](int s) {
#pragma unroll
    for (int nb = 0; nb < 4; ++nb) {
      const s16x4 lo = vtr((t0 ^ (32 * nb)) + (s % ONS) * 4096), hi = vtr((t0 ^ (32 * nb)) + (s % ONS) * 4096 + 512);
      wf[s & 1][nb] = PK8(lo, hi);
    }
  };
#pragma unroll
  for (int c = 0; c < ONS; ++c) fillb(c);
  int miss = -1;
  {
    const u64* fp = flags + ((tb * 8 + (mt >> 1)) * NREP + (nt & (NREP - 1))) * 32 + 4 * wid + (lane & 3);
    const u64 tp = __builtin_amdgcn_s_memrealtime();
    for (;;) {
      const u64 f = __hip_atomic_load(fp, __ATOMIC_RELAXED, __HIP_MEMORY_SCOPE_AGENT);
      const u64 now = __builtin_amdgcn_s_memrealtime();
      const u64 bad = __builtin_amdgcn_ballot_w64(!(f - tstart <= now - tstart + SLACK));
      if (bad == 0) break;
      if (now - tp > patience) { miss = 4 * wid + (__builtin_ctzll(bad) & 3); break; }
      __builtin_amdgcn_s_sleep(1);
    }
  }
  f32x4 acc[3][4] = {};
  if (miss < 0) {
#pragma unroll
    for (int c = 0; c < OPA; ++c) loada(c);
    wait_vm<4 * (ONS - 1) + 3 * OPA>();
    rdb(0);
#pragma unroll
    for (int s = 0; s < 16; ++s) {
      asm volatile("s_waitcnt vmcnt(%0) lgkmcnt(0)" :: "i"(oproj_wait(s)) : "memory");
      if (s < 15) {
        if (s + ONS < 16) fillb(s + ONS);
        rdb(s + 1);
      }
      asm volatile("" : "+v"(af[s % OPA][0]), "+v"(af[s % OPA][1]), "+v"(af[s % OPA][2]));
#pragma unroll
      for (int i = 0; i < 3; ++i)
#pragma unroll
        for (int nb = 0; nb < 4; ++nb) acc[i][nb] = MFMA16(wf[s & 1][nb], af[s % OPA][i], acc[i][nb]);
      __builtin_amdgcn_sched_barrier(0);
      if (s + OPA < 16) loada(s + OPA);
      __builtin_amdgcn_sched_barrier(0);
    }
  } else wait_vm<0>();
  {
    const unsigned wa = lb + g * 256 + 16 * ((lane & 15) ^ ((lane & 1) << 3) ^ (g >> 1));
#pragma unroll
    for (int i = 0; i < 3; ++i)
#pragma unroll
      for (int nb = 0; nb < 4; ++nb)
        *(__attribute__((address_space(3))) f32x4*)(uintptr_t)((wa ^ (32 * nb)) + (i * 4 + nb) * 1024) = acc[i][nb];
  }
  if (lane == 0) lds_st(lb + 12288, (unsigned)miss);
  bar();
  int mh = (int)lds_ld(lbase + (lane & 7) * (ONS * 4096) + 12288);
  const int row = tid >> 3, c8 = tid & 7;
  u32x4 ov = {0u, 0u, 0u, 0u};
  if (wid < 6) {
    f32x4 ps[16];
    const unsigned p = lbase + ((row >> 4) * 4 + (c8 >> 1)) * 1024 + (c8 & 1) * 512 + 16 * ((row & 15) ^ ((row & 1) << 3) ^ c8);
#pragma unroll
    for (int w = 0; w < 8; ++w) {
      ps[2 * w] = *(const __attribute__((address_space(3))) f32x4*)(uintptr_t)(p + w * (ONS * 4096));
      ps[2 * w + 1] = *(const __attribute__((address_space(3))) f32x4*)(uintptr_t)(p + w * (ONS * 4096) + 256);
    }
    asm volatile("" : "+v"(ps[0]), "+v"(ps[1]), "+v"(ps[2]), "+v"(ps[3]), "+v"(ps[4]), "+v"(ps[5]), "+v"(ps[6]), "+v"(ps[7]),
                      "+v"(ps[8]), "+v"(ps[9]), "+v"(ps[10]), "+v"(ps[11]), "+v"(ps[12]), "+v"(ps[13]), "+v"(ps[14]), "+v"(ps[15]));
    f32x4 s0 = ps[0], s1 = ps[1];
#pragma unroll
    for (int w = 1; w < 8; ++w) { s0 += ps[2 * w]; s1 += ps[2 * w + 1]; }
    ov = (u32x4){cvtpk(s0[0], s0[1]), cvtpk(s0[2], s0[3]), cvtpk(s1[0], s1[1]), cvtpk(s1[2], s1[3])};
  }
  {
    const u64 mb = __builtin_amdgcn_ballot_w64(mh >= 0);
    mh = mb ? __builtin_amdgcn_readlane(mh, __builtin_ctzll(mb)) : -1;
  }
  if (mh < 0) {
    if (wid < 6) *(u32x4*)(out + (size_t)(mt * 48 + row) * (2 * DM) + tb * DM + nt * 64 + 8 * c8) = ov;
    return;
  }
  item = (((mh & 7) * 4 + (mt >> 2)) << 3) + tb * 4 + (mh >> 3);
  patience = TMO1;
  bar();
  }
}

extern "C" void kernel_launch(void* const* d_in, const int* in_sizes, int n_in,
                              void* d_out, int out_size, void* d_ws, size_t ws_size,
                              hipStream_t stream) {
  const u16* xA = (const u16*)d_in[0]; const u16* kA = (const u16*)d_in[1]; const u16* vA = (const u16*)d_in[2];
  const u16* wqA = (const u16*)d_in[3]; const u16* woA = (const u16*)d_in[4];
  const u16* xB = (const u16*)d_in[5]; const u16* kB = (const u16*)d_in[6]; const u16* vB = (const u16*)d_in[7];
  const u16* wqB = (const u16*)d_in[8]; const u16* woB = (const u16*)d_in[9];
  u16* ows = (u16*)d_ws;
  attn_kernel<<<256, 512, 0, stream>>>(xA, kA, vA, wqA, xB, kB, vB, wqB, ows, woA, woB, (u16*)d_out);
}
```
